```python
import torch

def _softmax(x, axis=-1):
    return torch.softmax(x, dim=axis)
def _gelu(x):
    return torch.nn.functional.gelu(x, approximate="tanh")
def _silu(x):
    return torch.nn.functional.silu(x)
def _sigmoid(x):
    return torch.sigmoid(x)
def _layernorm(x, axis=-1, eps=1e-5):
    m = x.mean(dim=axis, keepdim=True)
    v = x.var(dim=axis, keepdim=True, unbiased=False)
    return (x - m) / torch.sqrt(v + eps)
def _scatter_add(tgt, idx, src, axis=0):
    return tgt.clone().index_add_(axis, idx, src)
def _segment_sum(src, seg, n):
    out = torch.zeros((n,) + src.shape[1:], dtype=src.dtype)
    return out.index_add_(0, seg, src)
def _pad(x, pw):
    flat = tuple(v for pair in reversed(pw) for v in pair)
    return torch.nn.functional.pad(x, flat)

kernel_name = 'parallel_sum__ssm_conv_s4__glu_gated_bilinear__prompt'

def setup_inputs():

    _w = (lambda a: (lambda t: t.to(torch.bfloat16) if t.is_floating_point() else t)(torch.as_tensor(a)))
    def _A_setup():
        B, L, D, N = 1, 2048, 448, 256
        A_re = -(lambda _x: torch.exp(torch.as_tensor(_x)))((lambda *s: torch.randn(*s, dtype=torch.bfloat16) * 6.4)(D, N))
        A_im = torch.pi * (lambda *s: torch.randn(*s, dtype=torch.bfloat16) * 0.05)(D, N)
        B_re = (lambda *s: torch.randn(*s, dtype=torch.bfloat16) * 0.1)(D, N)
        B_im = (lambda *s: torch.randn(*s, dtype=torch.bfloat16) * 0.1)(D, N)
        C_re = (lambda *s: torch.randn(*s, dtype=torch.bfloat16) * 0.1)(D, N)
        C_im = (lambda *s: torch.randn(*s, dtype=torch.bfloat16) * 0.1)(D, N)
        log_dt = (lambda *s: torch.randn(*s, dtype=torch.bfloat16) * 0.05)(D) * 0.5 - 4.0
        Dskip = (lambda *s: torch.randn(*s, dtype=torch.bfloat16) * 0.05)(D)
        return {
            "u": _w((lambda *s: torch.randn(*s, dtype=torch.bfloat16) * 0.05)(B, L, D)),
            "A_re": _w(A_re),
            "A_im": _w(A_im),
            "B_re": _w(B_re),
            "B_im": _w(B_im),
            "C_re": _w(C_re),
            "C_im": _w(C_im),
            "log_dt": _w(log_dt),
            "Dskip": _w(Dskip),
        }
    _A_in = _A_setup()

    def _B_setup():
        B, S, D, F = 1, 2048, 448, 1792
        return {
            "x": _w((lambda *s: torch.randn(*s, dtype=torch.bfloat16) * 0.05)(B, S, D)),
            "w_gate": _w((lambda *s: torch.randn(*s, dtype=torch.bfloat16) * 0.4)(D, F) * (D ** -0.5)),
            "w_up": _w((lambda *s: torch.randn(*s, dtype=torch.bfloat16) * 0.4)(D, F) * (D ** -0.5)),
            "w_down": _w((lambda *s: torch.randn(*s, dtype=torch.bfloat16) * 0.4)(F, D) * (F ** -0.5)),
        }
    _B_in = _B_setup()

    return {
        "A_u": _A_in["u"],
        "A_A_re": _A_in["A_re"],
        "A_A_im": _A_in["A_im"],
        "A_B_re": _A_in["B_re"],
        "A_B_im": _A_in["B_im"],
        "A_C_re": _A_in["C_re"],
        "A_C_im": _A_in["C_im"],
        "A_log_dt": _A_in["log_dt"],
        "A_Dskip": _A_in["Dskip"],
        "B_x": _B_in["x"],
        "B_w_gate": _B_in["w_gate"],
        "B_w_up": _B_in["w_up"],
        "B_w_down": _B_in["w_down"],
    }

def reference(A_u, A_A_re, A_A_im, A_B_re, A_B_im, A_C_re, A_C_im, A_log_dt, A_Dskip, B_x, B_w_gate, B_w_up, B_w_down):
    def _A_ref(u, A_re, A_im, B_re, B_im, C_re, C_im, log_dt, Dskip):
        Bsz, L, D = u.shape
        dt = torch.exp(log_dt)[:, None]
        A = A_re + 1j * A_im
        Bc = B_re + 1j * B_im
        Cc = C_re + 1j * C_im
        logdA = dt * A
        dB = dt * Bc
        l = (lambda x, dt: x.to(dt))(torch.arange(L), A_re.dtype)
        P = torch.exp(logdA[:, :, None] * l[None, None, :])
        K = torch.einsum("dn,dnl,dn->dl", Cc, P, dB).real
        L2 = 2 * L
        Kp = _pad(K, ((0, 0), (0, L)))
        up = (lambda x, *d: x.permute(*d))(u, 0, 2, 1)
        up = _pad(up, ((0, 0), (0, 0), (0, L)))
        Kf = (lambda x, axes=(-2,-1): torch.fft.rfft2(x.float(), dim=axes))(Kp[:, None, :], axes=(-2, -1))
        Uf = (lambda x, axes=(-2,-1): torch.fft.rfft2(x.float(), dim=axes))(up[:, :, None, :], axes=(-2, -1))
        Yf = Uf * Kf[None, :, :, :]
        y = (lambda x, s, axes=(-2,-1): torch.fft.irfft2(x, s=s, dim=axes).to(torch.bfloat16))(Yf, (1, L2), axes=(-2, -1))[:, :, 0, :L]
        y = (lambda x, *d: x.permute(*d))(y, 0, 2, 1) + u * Dskip[None, None, :]
        return y

    def _B_ref(x, w_gate, w_up, w_down):
        g = torch.einsum("bsd,df->bsf", x, w_gate)
        u = torch.einsum("bsd,df->bsf", x, w_up)
        h = g * u
        return torch.einsum("bsf,fd->bsd", h, w_down)

    x = A_u
    xn = _layernorm(x, axis=-1)
    return x + _A_ref(xn, A_A_re, A_A_im, A_B_re, A_B_im, A_C_re, A_C_im, A_log_dt, A_Dskip) + _B_ref(xn, B_w_gate, B_w_up, B_w_down)
```

```cpp
#include <hip/hip_runtime.h>
#include <hip/hip_bf16.h>
#include <stdint.h>

typedef unsigned short u16;
__device__ __forceinline__ float bf2f(u16 v){ return __uint_as_float(((unsigned)v)<<16); }
__device__ __forceinline__ u16 f2bf(float f){ unsigned u=__float_as_uint(f); u += 0x7fffu + ((u>>16)&1u); return (u16)(u>>16); }
__device__ __forceinline__ float rbf(float f){ return bf2f(f2bf(f)); }

constexpr int L=2048, D=448, N=256, F=1792;

__global__ void k_ln(const u16* x, u16* xn){
  int t=blockIdx.x; int lane=threadIdx.x;
  float v[7]; float s=0;
  for(int i=0;i<7;i++){ v[i]=bf2f(x[t*D+lane+64*i]); s+=v[i]; }
  for(int o=32;o>0;o>>=1) s+=__shfl_xor(s,o);
  float mean=s/(float)D; float q=0;
  for(int i=0;i<7;i++){ float d=v[i]-mean; q+=d*d; }
  for(int o=32;o>0;o>>=1) q+=__shfl_xor(q,o);
  float var=rbf(q/(float)D); float m=rbf(mean);
  float sd=rbf(sqrtf(rbf(var+1e-5f)));
  for(int i=0;i<7;i++){ float d=rbf(v[i]-m); xn[t*D+lane+64*i]=f2bf(d/sd); }
}
__global__ void k_kgen(const u16* Are,const u16* Aim,const u16* Bre,const u16* Bim,const u16* Cre,const u16* Cim,const u16* ldt,float* K){
  int d=blockIdx.y; int l=blockIdx.x*256+threadIdx.x;
  float dt=rbf(expf(bf2f(ldt[d])));
  float lf=rbf((float)l);
  float acc=0;
  for(int n=0;n<N;n++){
    float ar=dt*bf2f(Are[d*N+n]), ai=dt*bf2f(Aim[d*N+n]);
    float br=dt*bf2f(Bre[d*N+n]), bi=dt*bf2f(Bim[d*N+n]);
    float cr=bf2f(Cre[d*N+n]), ci=bf2f(Cim[d*N+n]);
    float wr=cr*br-ci*bi, wi=cr*bi+ci*br;
    float mag=expf(ar*lf); float sn,cs; sincosf(ai*lf,&sn,&cs);
    acc += mag*(wr*cs-wi*sn);
  }
  K[d*L+l]=acc;
}
__global__ void k_conv(const float* K,const u16* xn,const u16* Dskip,u16* aout){
  int d=blockIdx.y; int t=blockIdx.x*256+threadIdx.x;
  float acc=0;
  for(int l=0;l<=t;l++) acc+=K[d*L+l]*bf2f(xn[(t-l)*D+d]);
  float y=rbf(acc);
  float sk=rbf(bf2f(xn[t*D+d])*bf2f(Dskip[d]));
  aout[t*D+d]=f2bf(y+sk);
}
__global__ void k_g1(const u16* xn,const u16* wg,const u16* wu,u16* h){
  int t=blockIdx.y; int f=blockIdx.x*256+threadIdx.x;
  float g=0,u=0;
  for(int k=0;k<D;k++){ float a=bf2f(xn[t*D+k]); g+=a*bf2f(wg[k*F+f]); u+=a*bf2f(wu[k*F+f]); }
  h[t*F+f]=f2bf(rbf(g)*rbf(u));
}
__global__ void k_g2(const u16* h,const u16* wd,const u16* x,const u16* aout,u16* out){
  int t=blockIdx.x; int d=threadIdx.x;
  float acc=0;
  for(int k=0;k<F;k++) acc+=bf2f(h[t*F+k])*bf2f(wd[k*D+d]);
  float b=rbf(acc);
  float s=rbf(bf2f(x[t*D+d])+bf2f(aout[t*D+d]));
  out[t*D+d]=f2bf(s+b);
}

extern "C" void kernel_launch(void* const* d_in, const int* in_sizes, int n_in,
                              void* d_out, int out_size, void* d_ws, size_t ws_size,
                              hipStream_t stream) {
  const u16* x=(const u16*)d_in[0];
  char* ws=(char*)d_ws;
  u16* xn=(u16*)ws; float* K=(float*)(ws+(4<<20)); u16* aout=(u16*)(ws+(8<<20)); u16* h=(u16*)(ws+(12<<20));
  k_ln<<<L,64,0,stream>>>(x,xn);
  k_kgen<<<dim3(L/256,D),256,0,stream>>>((const u16*)d_in[1],(const u16*)d_in[2],(const u16*)d_in[3],(const u16*)d_in[4],(const u16*)d_in[5],(const u16*)d_in[6],(const u16*)d_in[7],K);
  k_conv<<<dim3(L/256,D),256,0,stream>>>(K,xn,(const u16*)d_in[8],aout);
  k_g1<<<dim3(F/256,L),256,0,stream>>>(xn,(const u16*)d_in[10],(const u16*)d_in[11],h);
  k_g2<<<L,D,0,stream>>>(h,(const u16*)d_in[12],x,aout,(u16*)d_out);
}
```

```cpp
#include <hip/hip_runtime.h>
#include <stdint.h>

typedef unsigned short u16;
typedef short bf16x8 __attribute__((ext_vector_type(8)));
typedef short s16x4 __attribute__((ext_vector_type(4)));
typedef float f32x16 __attribute__((ext_vector_type(16)));
typedef float f32x4 __attribute__((ext_vector_type(4)));
typedef float f32x2 __attribute__((ext_vector_type(2)));
typedef unsigned u32x4 __attribute__((ext_vector_type(4)));
typedef unsigned u32x2 __attribute__((ext_vector_type(2)));
typedef __bf16 bf16x2v __attribute__((ext_vector_type(2)));
#define LAS __attribute__((address_space(3)))
#define DI __device__ __forceinline__

constexpr int L_ = 2048, D_ = 448, N_ = 256, F_ = 1792;
constexpr int KL = 2112;

DI float bf2f(u16 v) { return __uint_as_float(((unsigned)v) << 16); }
DI unsigned cvtpk(float a, float b) { f32x2 p = {a, b}; return __builtin_bit_cast(unsigned, __builtin_convertvector(p, bf16x2v)); }
DI float rbf(float f) { return __uint_as_float(cvtpk(f, 0.f) << 16); }
DI float lo16(unsigned u) { return __uint_as_float(u << 16); }
DI float hi16(unsigned u) { return __uint_as_float(u & 0xffff0000u); }
DI float dppf(float v, const int ctrl_dummy);
#define DPPF(v, ctrl) __builtin_bit_cast(float, __builtin_amdgcn_mov_dpp(__builtin_bit_cast(int, (v)), (ctrl), 0xF, 0xF, true))
DI float red8(float v) { v += DPPF(v, 0xB1); v += DPPF(v, 0x4E); v += DPPF(v, 0x141); return v; }
#define MFMA32(a, b, c) __builtin_amdgcn_mfma_f32_32x32x16_bf16((a), (b), (c), 0, 0, 0)
#define MFMA16(a, b, c) __builtin_amdgcn_mfma_f32_16x16x32_bf16((a), (b), (c), 0, 0, 0)
#define WAITV(n) asm volatile("s_waitcnt vmcnt(" #n ")" ::: "memory")
#define WAITL(n) asm volatile("s_waitcnt lgkmcnt(" #n ")" ::: "memory")
#define BAR() do { asm volatile("" ::: "memory"); __builtin_amdgcn_s_barrier(); asm volatile("" ::: "memory"); } while (0)

constexpr size_t WS_H = 0;
constexpr size_t WS_WD = WS_H + (size_t)64 * 112 * 1024;
constexpr size_t WS_XT = WS_WD + (size_t)14 * 112 * 1024;
constexpr size_t WS_K = WS_XT + (size_t)D_ * L_ * 2;
constexpr size_t WS_AT = WS_K + (size_t)D_ * 2 * KL * 2;
constexpr size_t WS_END = WS_AT + (size_t)D_ * L_ * 2;

constexpr int A_STRIDE = 912;
constexpr int G1_A_BYTES = 128 * A_STRIDE;
constexpr int G1_B_OFF = G1_A_BYTES;
constexpr int G1_LDS = G1_B_OFF + 4 * 8192;

__global__ __launch_bounds__(512, 2) void k_gemm1(const u16* __restrict__ x, const u16* __restrict__ wg, const u16* __restrict__ wu,
                                                   u16* __restrict__ hfrag, u16* __restrict__ xnT) {
  extern __shared__ __attribute__((aligned(16))) char sm[];
  const int tid = threadIdx.x, lane = tid & 63;
  const int wave = __builtin_amdgcn_readfirstlane(tid >> 6);
  const int b = blockIdx.x, xcd = b & 7, idx = b >> 3;
  const int mt = (xcd & 3) * 4 + (idx & 3), nt = (xcd >> 2) * 7 + (idx >> 2);

  const int mtx = (wave >> 1) & 1, half = wave & 1;
  const u16* wsrc = (mtx ? wu : wg) + (size_t)nt * 128;
  const u16* src0;
  const u16* src1;
  {
    int j0 = 2 * half, j1 = j0 + 1;
    int r0 = 4 * j0 + (lane >> 4), r1 = 4 * j1 + (lane >> 4);
    int c0 = (lane & 15) ^ (((lane >> 4) << 2) | j0), c1 = (lane & 15) ^ (((lane >> 4) << 2) | j1);
    src0 = wsrc + (size_t)r0 * F_ + c0 * 8;
    src1 = wsrc + (size_t)r1 * F_ + c1 * 8;
  }
  char* bdst = sm + G1_B_OFF + mtx * 4096 + half * 2048;
  auto stage = [&](int s) {
    __builtin_amdgcn_global_load_lds((const unsigned*)(src0 + (size_t)s * 16 * F_), (unsigned*)(bdst + (s & 3) * 8192), 16, 0, 0);
    __builtin_amdgcn_global_load_lds((const unsigned*)(src1 + (size_t)s * 16 * F_), (unsigned*)(bdst + (s & 3) * 8192 + 1024), 16, 0, 0);
  };
  if (wave < 4) { stage(0); stage(1); stage(2); stage(3); }

  {
    const int sub = lane & 7;
    u32x4 raw[2][7];
#pragma unroll
    for (int p = 0; p < 2; ++p) {
      const int row = 16 * wave + 8 * p + (lane >> 3);
      const u32x4* xp = (const u32x4*)(x + (size_t)(mt * 128 + row) * D_);
#pragma unroll
      for (int j = 0; j < 7; ++j) raw[p][j] = xp[sub + 8 * j];
    }
#pragma unroll
    for (int p = 0; p < 2; ++p) {
      const int row = 16 * wave + 8 * p + (lane >> 3);
      f32x2 xf[28];
      f32x2 s2 = {0.f, 0.f};
#pragma unroll
      for (int j = 0; j < 7; ++j)
#pragma unroll
        for (int e = 0; e < 4; ++e) { unsigned u = raw[p][j][e]; f32x2 v = {lo16(u), hi16(u)}; xf[j * 4 + e] = v; s2 += v; }
      float mean = red8(s2.x + s2.y) / 448.0f;
      float m = rbf(mean);
      f32x2 q2 = {0.f, 0.f};
      f32x2 mm = {m, m};
#pragma unroll
      for (int i = 0; i < 28; ++i) { f32x2 d = xf[i] - mm; xf[i] = d; q2 += d * d; }
      float dl = mean - m;
      float var = red8(q2.x + q2.y) / 448.0f - dl * dl;
      var = fmaxf(var, 0.f);
      float sd = rbf(sqrtf(rbf(rbf(var) + 1e-5f)));
      float r = 1.0f / sd;
      f32x2 rr = {r, r};
#pragma unroll
      for (int j = 0; j < 7; ++j) {
        u32x4 o;
#pragma unroll
        for (int e = 0; e < 4; ++e) {
          unsigned pk = cvtpk(xf[j * 4 + e].x, xf[j * 4 + e].y);
          f32x2 v = {lo16(pk), hi16(pk)};
          v = v * rr;
          o[e] = cvtpk(v.x, v.y);
        }
        *(u32x4*)(sm + row * A_STRIDE + (sub + 8 * j) * 16) = o;
      }
    }
  }
  __syncthreads();

  if (wave >= 4) {
    const int ht = tid - 256;
#pragma unroll
    for (int rep = 0; rep < 2; ++rep) {
      const int id = ht + 256 * rep;
      const int c = id >> 4, tq = id & 15;
      unsigned v[8];
#pragma unroll
      for (int i = 0; i < 8; ++i) v[i] = *(const u16*)(sm + (8 * tq + i) * A_STRIDE + (32 * nt + c) * 2);
      u32x4 o = {v[0] | (v[1] << 16), v[2] | (v[3] << 16), v[4] | (v[5] << 16), v[6] | (v[7] << 16)};
      *(u32x4*)(xnT + (size_t)(32 * nt + c) * L_ + mt * 128 + 8 * tq) = o;
    }
    for (int s = 0; s < 27; ++s) BAR();
    return;
  }

  const int wt = wave & 1, wf = wave >> 1;
  const int h = lane >> 5, blk = (lane >> 4) & 1, q = (lane & 15) >> 2, p = lane & 3;
  unsigned tra[2][2];
#pragma unroll
  for (int cc = 0; cc < 2; ++cc)
#pragma unroll
    for (int t = 0; t < 2; ++t) {
      int c = 2 * wf + cc, row = 8 * h + 4 * t + q;
      tra[cc][t] = G1_B_OFF + 256 * row + 16 * (((c ^ q) << 2) | ((2 * blk + (p >> 1)) ^ (2 * h + t))) + 8 * (p & 1);
    }
  const unsigned xa0 = (64 * wt + (lane & 31)) * A_STRIDE + 16 * h;

  f32x16 acc[2][2][2];
#pragma unroll
  for (int a = 0; a < 2; ++a)
#pragma unroll
    for (int c = 0; c < 2; ++c)
#pragma unroll
      for (int g = 0; g < 2; ++g)
#pragma unroll
        for (int i = 0; i < 16; ++i) acc[a][c][g][i] = 0.f;

  bf16x8 xf[2][2], wf_[2][2][2];
  auto rd = [&](int s, int buf) {
#pragma unroll
    for (int tt = 0; tt < 2; ++tt) xf[buf][tt] = *(const bf16x8*)(sm + xa0 + tt * 32 * A_STRIDE + s * 32);
#pragma unroll
    for (int g = 0; g < 2; ++g)
#pragma unroll
      for (int cc = 0; cc < 2; ++cc) {
        s16x4 a0 = __builtin_amdgcn_ds_read_tr16_b64_v4i16((LAS s16x4*)(sm + tra[cc][0] + (s & 3) * 8192 + g * 4096));
        s16x4 a1 = __builtin_amdgcn_ds_read_tr16_b64_v4i16((LAS s16x4*)(sm + tra[cc][1] + (s & 3) * 8192 + g * 4096));
        wf_[buf][g][cc] = __builtin_shufflevector(a0, a1, 0, 1, 2, 3, 4, 5, 6, 7);
      }
  };
  rd(0, 0);
#pragma unroll
  for (int s = 0; s < 28; ++s) {
    if (s <= 26) {
      if (s <= 24) WAITV(4); else if (s == 25) WAITV(2); else WAITV(0);
      WAITL(0);
      BAR();
      if (s + 4 <= 27) stage(s + 4);
      rd(s + 1, (s + 1) & 1);
    }
#pragma unroll
    for (int tt = 0; tt < 2; ++tt)
#pragma unroll
      for (int cc = 0; cc < 2; ++cc)
#pragma unroll
        for (int g = 0; g < 2; ++g) acc[tt][cc][g] = MFMA32(wf_[s & 1][g][cc], xf[s & 1][tt], acc[tt][cc][g]);
  }
#pragma unroll
  for (int tt = 0; tt < 2; ++tt)
#pragma unroll
    for (int cc = 0; cc < 2; ++cc)
#pragma unroll
      for (int s = 0; s < 2; ++s) {
        u32x4 o;
#pragma unroll
        for (int e = 0; e < 4; ++e) {
          unsigned gp = cvtpk(acc[tt][cc][0][8 * s + 2 * e], acc[tt][cc][0][8 * s + 2 * e + 1]);
          unsigned up = cvtpk(acc[tt][cc][1][8 * s + 2 * e], acc[tt][cc][1][8 * s + 2 * e + 1]);
          o[e] = cvtpk(lo16(gp) * lo16(up), hi16(gp) * hi16(up));
        }
        const int mtile = 4 * mt + 2 * wt + tt, kstep = 8 * nt + 4 * wf + 2 * cc + s;
        *(u32x4*)((char*)hfrag + ((size_t)(mtile * 112 + kstep) * 64 + lane) * 16) = o;
      }
}

DI f32x2 cmul(f32x2 a, f32x2 b) {
  f32x2 r;
  r.x = a.x * b.x - a.y * b.y;
  r.y = a.x * b.y + a.y * b.x;
  return r;
}
DI f32x2 cexp_rev(float ar2, float ai2, float tau) {
  float m = __builtin_amdgcn_exp2f(ar2 * tau);
  float fr = __builtin_amdgcn_fractf(ai2 * tau);
  f32x2 r = {m * __builtin_amdgcn_cosf(fr), m * __builtin_amdgcn_sinf(fr)};
  return r;
}
DI void split2(f32x2 v, unsigned& hi, unsigned& lo) {
  hi = cvtpk(v.x, v.y);
  f32x2 hv = {lo16(hi), hi16(hi)};
  f32x2 d = v - hv;
  lo = cvtpk(d.x, d.y);
}

__global__ __launch_bounds__(256) void k_kgen(const u16* __restrict__ Are, const u16* __restrict__ Aim, const u16* __restrict__ Bre,
                                              const u16* __restrict__ Bim, const u16* __restrict__ Cre, const u16* __restrict__ Cim,
                                              const u16* __restrict__ ldt, u16* __restrict__ Kg) {
  __shared__ __attribute__((aligned(16))) char sm[16384 + 4096];
  const int d = blockIdx.x, tid = threadIdx.x, lane = tid & 63, wq = tid >> 6;
  char* wbase = sm + wq * 4096;
  {
    const int n = d * N_ + tid;
    const float dt = rbf(expf(bf2f(ldt[d])));
    float ar = dt * bf2f(Are[n]), ai = dt * bf2f(Aim[n]);
    float br = dt * bf2f(Bre[n]), bi = dt * bf2f(Bim[n]);
    float cr = bf2f(Cre[n]), ci = bf2f(Cim[n]);
    float wr = cr * br - ci * bi, wi = cr * bi + ci * br;
    float ar2 = ar * 1.4426950408889634f, ai2 = ai * 0.15915494309189535f;
    f32x2 z4 = cexp_rev(ar2, ai2, 4.f), z128 = cexp_rev(ar2, ai2, 128.f);
    f32x4 p0 = {ar2, ai2, wr, -wi}, p1 = {z4.x, -z4.y, z128.x, z128.y};
    *(f32x4*)(wbase + lane * 32) = p0;
    *(f32x4*)(wbase + lane * 32 + 16) = p1;
  }
  __builtin_amdgcn_wave_barrier();
  asm volatile("s_waitcnt lgkmcnt(0)" ::: "memory");
  const int row = lane & 15, g = lane >> 4;
  float tauA;
  {
    int t = row < 4 ? row : (row == 4 ? 256 : (row == 5 ? 258 : (row <= 12 ? (row - 4) * 256 : 0)));
    tauA = (float)t;
  }
  const float tauB = (float)(8 * row);
  f32x4 acc[2][2];
#pragma unroll
  for (int a = 0; a < 2; ++a)
#pragma unroll
    for (int bb = 0; bb < 2; ++bb) acc[a][bb] = f32x4{0.f, 0.f, 0.f, 0.f};
#pragma unroll
  for (int ks = 0; ks < 4; ++ks) {
    u32x4 Ah[2], Al[2], Bh[2], Bl[2];
#pragma unroll
    for (int i = 0; i < 4; ++i) {
      const int nl = 16 * ks + 4 * g + i;
      f32x4 p0 = *(const f32x4*)(wbase + nl * 32), p1 = *(const f32x4*)(wbase + nl * 32 + 16);
      f32x2 pa = cexp_rev(p0.x, -p0.y, tauA);
      f32x2 u0 = cmul(f32x2{p0.z, p0.w}, pa);
      f32x2 u1 = cmul(u0, f32x2{p1.x, p1.y});
      f32x2 v0 = cexp_rev(p0.x, p0.y, tauB);
      f32x2 v1 = cmul(v0, f32x2{p1.z, p1.w});
      unsigned hh, ll;
      split2(u0, hh, ll); Ah[0][i] = hh; Al[0][i] = ll;
      split2(u1, hh, ll); Ah[1][i] = hh; Al[1][i] = ll;
      split2(v0, hh, ll); Bh[0][i] = hh; Bl[0][i] = ll;
      split2(v1, hh, ll); Bh[1][i] = hh; Bl[1][i] = ll;
    }
#pragma unroll
    for (int a = 0; a < 2; ++a)
#pragma unroll
      for (int bb = 0; bb < 2; ++bb) {
        acc[a][bb] = MFMA16(__builtin_bit_cast(bf16x8, Al[a]), __builtin_bit_cast(bf16x8, Bh[bb]), acc[a][bb]);
        acc[a][bb] = MFMA16(__builtin_bit_cast(bf16x8, Ah[a]), __builtin_bit_cast(bf16x8, Bl[bb]), acc[a][bb]);
        acc[a][bb] = MFMA16(__builtin_bit_cast(bf16x8, Ah[a]), __builtin_bit_cast(bf16x8, Bh[bb]), acc[a][bb]);
      }
  }
  __builtin_amdgcn_wave_barrier();
#pragma unroll
  for (int a = 0; a < 2; ++a)
#pragma unroll
    for (int bb = 0; bb < 2; ++bb) *(f32x4*)(wbase + (a * 2 + bb) * 1024 + lane * 16) = acc[a][bb];
  __syncthreads();
  {
    f32x4 s = *(const f32x4*)(sm + tid * 16);
    s += *(const f32x4*)(sm + 4096 + tid * 16);
    s += *(const f32x4*)(sm + 8192 + tid * 16);
    s += *(const f32x4*)(sm + 12288 + tid * 16);
    *(f32x4*)(sm + 16384 + tid * 16) = s;
  }
  __syncthreads();
  {
    const float* tab = (const float*)(sm + 16384);
    float kv[8];
#pragma unroll
    for (int e = 0; e < 8; ++e) {
      const int l = 2047 - (8 * tid + e);
      const int v = (int)rbf((float)l);
      int rw, a, cp;
      if (v < 256) { rw = v & 3; a = (v >> 2) & 1; cp = v >> 3; }
      else if (v < 512) { int w = v - 256; rw = 4 + ((w >> 1) & 1); a = (w >> 2) & 1; cp = w >> 3; }
      else if (v < 1024) { int w = v - 512; rw = 6 + (w >> 8); a = (w >> 2) & 1; cp = (w & 255) >> 3; }
      else if (v < 2048) { int w = v - 1024; rw = 8 + (w >> 8); a = 0; cp = (w & 255) >> 3; }
      else { rw = 12; a = 0; cp = 0; }
      const int bb = cp >> 4, col = cp & 15;
      kv[e] = tab[((a * 2 + bb) * 64 + col + 16 * (rw >> 2)) * 4 + (rw & 3)];
    }
    u32x4 oh, ol;
#pragma unroll
    for (int e = 0; e < 4; ++e) { unsigned hh, ll; split2(f32x2{kv[2 * e], kv[2 * e + 1]}, hh, ll); oh[e] = hh; ol[e] = ll; }
    u16* kh = Kg + (size_t)d * 2 * KL;
    *(u32x4*)(kh + 8 * tid) = oh;
    *(u32x4*)(kh + KL + 8 * tid) = ol;
    if (tid < 16) { u32x4 z = {0, 0, 0, 0}; *(u32x4*)(kh + (tid >> 3) * KL + 2048 + 8 * (tid & 7)) = z; }
  }
}

__global__ __launch_bounds__(256) void k_retile(const u16* __restrict__ wd, u16* __restrict__ wdf) {
  const int lane = threadIdx.x & 63, fr = blockIdx.x * 4 + (threadIdx.x >> 6);
  const int nt = fr / 112, ks = fr % 112, h = lane >> 5;
  unsigned v[8];
#pragma unroll
  for (int j = 0; j < 8; ++j) v[j] = wd[(size_t)(16 * ks + 8 * (j >> 2) + 4 * h + (j & 3)) * D_ + 32 * nt + (lane & 31)];
  u32x4 o = {v[0] | (v[1] << 16), v[2] | (v[3] << 16), v[4] | (v[5] << 16), v[6] | (v[7] << 16)};
  *(u32x4*)((char*)wdf + ((size_t)fr * 64 + lane) * 16) = o;
}

constexpr int CS = 4160;
constexpr int CV_X_OFF = 8 * CS;
constexpr int CV_LDS = CV_X_OFF + 96 * 80;

__global__ __launch_bounds__(256) void k_conv(const u16* __restrict__ Kg, const u16* __restrict__ xnT, const u16* __restrict__ Dskip,
                                              u16* __restrict__ aT) {
  extern __shared__ __attribute__((aligned(16))) char sm[];
  const int d = blockIdx.x, tid = threadIdx.x, lane = tid & 63;
  const int wave = __builtin_amdgcn_readfirstlane(tid >> 6);
  {
    const char* src = (const char*)(Kg + (size_t)d * 2 * KL) + 2 * wave + lane * 16;
#pragma unroll
    for (int hl = 0; hl < 2; ++hl) {
#pragma unroll
      for (int j = 0; j < 4; ++j)
        __builtin_amdgcn_global_load_lds((const unsigned*)(src + hl * (KL * 2) + j * 1024), (unsigned*)(sm + (hl * 4 + wave) * CS + j * 1024), 16, 0, 0);
      if (lane < 4)
        __builtin_amdgcn_global_load_lds((const unsigned*)(src + hl * (KL * 2) + 4096), (unsigned*)(sm + (hl * 4 + wave) * CS + 4096), 16, 0, 0);
    }
    u32x4 xv = *(const u32x4*)(xnT + (size_t)d * L_ + tid * 8);
    *(u32x4*)(sm + CV_X_OFF + (32 + (tid >> 2)) * 80 + (tid & 3) * 16) = xv;
    if (tid < 160) { u32x4 z = {0, 0, 0, 0}; *(u32x4*)(sm + CV_X_OFF + tid * 16) = z; }
  }
  WAITV(0);
  __syncthreads();

  const int r = lane & 31, h = lane >> 5;
  const int cls = (3 - r) & 3;
  const unsigned abase = cls * CS + 2 * (2047 - 32 * (60 + wave) - r + 8 * h - cls);
  const unsigned bbase = CV_X_OFF + (64 + r - (60 + wave)) * 80 + 16 * h;
  f32x16 acc0, acc1;
#pragma unroll
  for (int i = 0; i < 16; ++i) { acc0[i] = 0.f; acc1[i] = 0.f; }
#pragma unroll
  for (int q = 0; q < 16; ++q) {
#pragma unroll
    for (int kk = 0; kk < 2; ++kk) {
      const unsigned ao = abase + 256 * (15 - q) + 32 * kk;
      s16x4 h0 = *(const s16x4*)(sm + ao), h1 = *(const s16x4*)(sm + ao + 8);
      s16x4 l0 = *(const s16x4*)(sm + ao + 4 * CS), l1 = *(const s16x4*)(sm + ao + 4 * CS + 8);
      bf16x8 Ah = __builtin_shufflevector(h0, h1, 0, 1, 2, 3, 4, 5, 6, 7);
      bf16x8 Al = __builtin_shufflevector(l0, l1, 0, 1, 2, 3, 4, 5, 6, 7);
      const unsigned bo = bbase + 320 * (15 - q) + 32 * kk;
      bf16x8 B1 = *(const bf16x8*)(sm + bo);
      acc1 = MFMA32(Ah, B1, acc1);
      acc1 = MFMA32(Al, B1, acc1);
      if (q < 8) {
        bf16x8 B0 = *(const bf16x8*)(sm + bo - 32 * 80);
        acc0 = MFMA32(Ah, B0, acc0);
        acc0 = MFMA32(Al, B0, acc0);
      }
    }
  }
  __syncthreads();
#pragma unroll
  for (int g = 0; g < 4; ++g) {
    *(f32x4*)(sm + wave * 8192 + g * 1024 + lane * 16) = f32x4{acc0[4 * g], acc0[4 * g + 1], acc0[4 * g + 2], acc0[4 * g + 3]};
    *(f32x4*)(sm + wave * 8192 + 4096 + g * 1024 + lane * 16) = f32x4{acc1[4 * g], acc1[4 * g + 1], acc1[4 * g + 2], acc1[4 * g + 3]};
  }
  __syncthreads();
  {
    const int i = tid >> 2, g = tid & 3, tile = i >> 5, col = i & 31;
    f32x4 s0 = {0.f, 0.f, 0.f, 0.f}, s1 = {0.f, 0.f, 0.f, 0.f};
#pragma unroll
    for (int w = 0; w < 4; ++w) {
      s0 += *(const f32x4*)(sm + w * 8192 + tile * 4096 + g * 1024 + col * 16);
      s1 += *(const f32x4*)(sm + w * 8192 + tile * 4096 + g * 1024 + (col + 32) * 16);
    }
    u32x4 xv = *(const u32x4*)(sm + CV_X_OFF + (32 + i) * 80 + g * 16);
    const float ds = bf2f(Dskip[d]);
    float y[8] = {s0.x, s0.y, s0.z, s0.w, s1.x, s1.y, s1.z, s1.w};
    u32x4 o;
#pragma unroll
    for (int e = 0; e < 4; ++e) {
      unsigned yp = cvtpk(y[2 * e], y[2 * e + 1]);
      unsigned sp = cvtpk(lo16(xv[e]) * ds, hi16(xv[e]) * ds);
      o[e] = cvtpk(lo16(yp) + lo16(sp), hi16(yp) + hi16(sp));
    }
    *(u32x4*)(aT + (size_t)d * L_ + tid * 8) = o;
  }
}

constexpr int G2_LDS = 49152;
__global__ __launch_bounds__(256) void k_gemm2(const u16* __restrict__ hfrag, const u16* __restrict__ wdf, const u16* __restrict__ x,
                                               const u16* __restrict__ aT, u16* __restrict__ out) {
  extern __shared__ __attribute__((aligned(16))) char sm[];
  const int tid = threadIdx.x, lane = tid & 63;
  const int wave = __builtin_amdgcn_readfirstlane(tid >> 6);
  const int b = blockIdx.x, xcd = b & 7, idx = b >> 3;
  const int mt2 = xcd * 4 + (idx & 3), nt2 = idx >> 2;
  const u32x4* hp = (const u32x4*)((const char*)hfrag + ((size_t)(2 * mt2) * 112 + 28 * wave) * 1024) + lane;
  const u32x4* wp = (const u32x4*)((const char*)wdf + ((size_t)(2 * nt2) * 112 + 28 * wave) * 1024) + lane;
  f32x16 acc[2][2];
#pragma unroll
  for (int i = 0; i < 2; ++i)
#pragma unroll
    for (int j = 0; j < 2; ++j)
#pragma unroll
      for (int e = 0; e < 16; ++e) acc[i][j][e] = 0.f;
#pragma unroll
  for (int ks = 0; ks < 28; ++ks) {
    u32x4 h0 = hp[ks * 64], h1 = hp[(112 + ks) * 64];
    u32x4 w0 = wp[ks * 64], w1 = wp[(112 + ks) * 64];
    acc[0][0] = MFMA32(__builtin_bit_cast(bf16x8, w0), __builtin_bit_cast(bf16x8, h0), acc[0][0]);
    acc[0][1] = MFMA32(__builtin_bit_cast(bf16x8, w1), __builtin_bit_cast(bf16x8, h0), acc[0][1]);
    acc[1][0] = MFMA32(__builtin_bit_cast(bf16x8, w0), __builtin_bit_cast(bf16x8, h1), acc[1][0]);
    acc[1][1] = MFMA32(__builtin_bit_cast(bf16x8, w1), __builtin_bit_cast(bf16x8, h1), acc[1][1]);
  }
#pragma unroll
  for (int tl = 0; tl < 4; ++tl) {
    if (tl != wave) {
      const int slot = wave < tl ? wave : wave - 1;
#pragma unroll
      for (int g = 0; g < 4; ++g)
        *(f32x4*)(sm + (tl * 3 + slot) * 4096 + g * 1024 + lane * 16) =
            f32x4{acc[tl & 1][tl >> 1][4 * g], acc[tl & 1][tl >> 1][4 * g + 1], acc[tl & 1][tl >> 1][4 * g + 2], acc[tl & 1][tl >> 1][4 * g + 3]};
    }
  }
  __syncthreads();
  f32x16 own;
  if (wave == 0) own = acc[0][0]; else if (wave == 1) own = acc[1][0]; else if (wave == 2) own = acc[0][1]; else own = acc[1][1];
  float sum[16];
#pragma unroll
  for (int e = 0; e < 16; ++e) sum[e] = 0.f;
#pragma unroll
  for (int sw = 0; sw < 4; ++sw) {
    if (sw == wave) {
#pragma unroll
      for (int e = 0; e < 16; ++e) sum[e] += own[e];
    } else {
      const int slot = sw < wave ? sw : sw - 1;
#pragma unroll
      for (int g = 0; g < 4; ++g) {
        f32x4 v = *(const f32x4*)(sm + (wave * 3 + slot) * 4096 + g * 1024 + lane * 16);
        sum[4 * g] += v.x; sum[4 * g + 1] += v.y; sum[4 * g + 2] += v.z; sum[4 * g + 3] += v.w;
      }
    }
  }
  const int t = 64 * mt2 + 32 * (wave & 1) + (lane & 31), h = lane >> 5;
  const int d0 = 64 * nt2 + 32 * (wave >> 1) + 4 * h;
#pragma unroll
  for (int g = 0; g < 4; ++g) {
    const int dd = d0 + 8 * g;
    u32x2 xv = *(const u32x2*)(x + (size_t)t * D_ + dd);
    float a0 = bf2f(aT[(size_t)dd * L_ + t]), a1 = bf2f(aT[(size_t)(dd + 1) * L_ + t]);
    float a2 = bf2f(aT[(size_t)(dd + 2) * L_ + t]), a3 = bf2f(aT[(size_t)(dd + 3) * L_ + t]);
    unsigned s01 = cvtpk(lo16(xv.x) + a0, hi16(xv.x) + a1), s23 = cvtpk(lo16(xv.y) + a2, hi16(xv.y) + a3);
    unsigned m01 = cvtpk(sum[4 * g], sum[4 * g + 1]), m23 = cvtpk(sum[4 * g + 2], sum[4 * g + 3]);
    u32x2 o = {cvtpk(lo16(s01) + lo16(m01), hi16(s01) + hi16(m01)), cvtpk(lo16(s23) + lo16(m23), hi16(s23) + hi16(m23))};
    *(u32x2*)(out + (size_t)t * D_ + dd) = o;
  }
}

extern "C" void kernel_launch(void* const* d_in, const int* in_sizes, int n_in, void* d_out, int out_size, void* d_ws, size_t ws_size,
                              hipStream_t stream) {
  const u16* x = (const u16*)d_in[0];
  char* ws = (char*)d_ws;
  u16* hfrag = (u16*)(ws + WS_H);
  u16* wdf = (u16*)(ws + WS_WD);
  u16* xnT = (u16*)(ws + WS_XT);
  u16* Kg = (u16*)(ws + WS_K);
  u16* aT = (u16*)(ws + WS_AT);
  hipFuncSetAttribute((const void*)k_gemm1, hipFuncAttributeMaxDynamicSharedMemorySize, G1_LDS);
  k_gemm1<<<224, 512, G1_LDS, stream>>>(x, (const u16*)d_in[10], (const u16*)d_in[11], hfrag, xnT);
  k_kgen<<<448, 256, 0, stream>>>((const u16*)d_in[1], (const u16*)d_in[2], (const u16*)d_in[3], (const u16*)d_in[4], (const u16*)d_in[5],
                                  (const u16*)d_in[6], (const u16*)d_in[7], Kg);
  k_retile<<<392, 256, 0, stream>>>((const u16*)d_in[12], wdf);
  k_conv<<<448, 256, CV_LDS, stream>>>(Kg, xnT, (const u16*)d_in[8], aT);
  k_gemm2<<<224, 256, G2_LDS, stream>>>(hfrag, wdf, x, aT, (u16*)d_out);
}
```

```cpp
#include <hip/hip_runtime.h>
#include <stdint.h>

typedef unsigned short u16;
typedef short bf16x8 __attribute__((ext_vector_type(8)));
typedef short s16x4 __attribute__((ext_vector_type(4)));
typedef float f32x16 __attribute__((ext_vector_type(16)));
typedef float f32x4 __attribute__((ext_vector_type(4)));
typedef float f32x2 __attribute__((ext_vector_type(2)));
typedef unsigned u32x4 __attribute__((ext_vector_type(4)));
typedef unsigned u32x2 __attribute__((ext_vector_type(2)));
typedef __bf16 bf16x2v __attribute__((ext_vector_type(2)));
#define LAS __attribute__((address_space(3)))
#define GAS __attribute__((address_space(1)))
#define DI __device__ __forceinline__

constexpr int L_ = 2048, D_ = 448, N_ = 256, F_ = 1792;
constexpr int KL = 2112;

DI float bf2f(u16 v) { return __uint_as_float(((unsigned)v) << 16); }
DI unsigned cvtpk(float a, float b) { f32x2 p = {a, b}; return __builtin_bit_cast(unsigned, __builtin_convertvector(p, bf16x2v)); }
DI float rbf(float f) { return __uint_as_float(cvtpk(0.f, f)); }
DI float lo16(unsigned u) { return __uint_as_float(u << 16); }
DI float hi16(unsigned u) { return __uint_as_float(u & 0xffff0000u); }
#define DPPF(v, ctrl) __builtin_bit_cast(float, __builtin_amdgcn_mov_dpp(__builtin_bit_cast(int, (v)), (ctrl), 0xF, 0xF, true))
DI float red8(float v) { v += DPPF(v, 0xB1); v += DPPF(v, 0x4E); v += DPPF(v, 0x141); return v; }
#define MFMA32(a, b, c) __builtin_amdgcn_mfma_f32_32x32x16_bf16((a), (b), (c), 0, 0, 0)
#define MFMA16(a, b, c) __builtin_amdgcn_mfma_f32_16x16x32_bf16((a), (b), (c), 0, 0, 0)
#define WAITV(n) asm volatile("s_waitcnt vmcnt(" #n ")" ::: "memory")
#define WAITL(n) asm volatile("s_waitcnt lgkmcnt(" #n ")" ::: "memory")
#define SB() __builtin_amdgcn_sched_barrier(0)

#define STAMP(k, i)
#define RSTAMP(k, i)

constexpr size_t WS_CNT = 0;
constexpr size_t WS_H = 4096;
constexpr size_t WS_WD = WS_H + (size_t)64 * 112 * 1024;
constexpr size_t WS_XT = WS_WD + (size_t)14 * 112 * 1024;
constexpr size_t WS_K = WS_XT + (size_t)D_ * L_ * 2;
constexpr size_t WS_AT = WS_K + (size_t)D_ * 2 * KL * 2;

struct TapTab { u16 v[2048]; };
constexpr TapTab make_taptab() {
  TapTab t{};
  for (int i = 0; i < 2048; ++i) {
    int l = 2047 - i;
    int sh = l < 256 ? 0 : (l < 512 ? 1 : (l < 1024 ? 2 : 3));
    int v = l;
    if (sh) {
      int q = l >> sh, r = l & ((1 << sh) - 1), hf = 1 << (sh - 1);
      if (r > hf || (r == hf && (q & 1))) q += 1;
      v = q << sh;
    }
    int cp = (v & 255) >> 3, a = (v >> 2) & 1, h8 = v >> 8;
    int row = h8 == 0 ? (v & 3) : (h8 == 1 ? 4 + ((v >> 1) & 1) : 4 + h8);
    int bb = cp >> 4, col = cp & 15, ln = col + 16 * (row >> 2);
    int off = (a == 0 ? bb * 1024 : 2048 + bb * 512) + ln * 16 + (row & 3) * 4;
    t.v[i] = (u16)off;
  }
  return t;
}
__device__ const TapTab g_taptab = make_taptab();

DI f32x2 cmul(f32x2 a, f32x2 b) {
  f32x2 t, r;
  asm("v_pk_mul_f32 %0, %1, %2 op_sel:[0,0] op_sel_hi:[0,1]" : "=v"(t) : "v"(a), "v"(b));
  asm("v_pk_fma_f32 %0, %1, %2, %3 op_sel:[1,1,0] op_sel_hi:[1,0,1] neg_lo:[1,0,0]" : "=v"(r) : "v"(a), "v"(b), "v"(t));
  return r;
}
DI f32x2 cexp2(f32x2 ep) {
  float m = __builtin_amdgcn_exp2f(ep.x);
  float fr = __builtin_amdgcn_fractf(ep.y);
  f32x2 cs = {__builtin_amdgcn_cosf(fr), __builtin_amdgcn_sinf(fr)};
  f32x2 mm = {m, m};
  return cs * mm;
}
DI void split2(f32x2 v, unsigned& hi, unsigned& lo) {
  hi = cvtpk(v.x, v.y);
  f32x2 hv = {lo16(hi), hi16(hi)};
  f32x2 d = v - hv;
  lo = cvtpk(d.x, d.y);
}

constexpr int A_STRIDE = 912;
constexpr int G1_A_BYTES = 128 * A_STRIDE;
constexpr int G1_B_OFF = G1_A_BYTES;
constexpr int G1_K_OFF = G1_B_OFF + 32768;
constexpr int G1_LDS = G1_K_OFF + 14336;

struct KParams { f32x4 p0, p1; };
DI KParams kparams(float dt, u16 are, u16 aim, u16 bre, u16 bim, u16 cre, u16 cim) {
  float ar = dt * bf2f(are), ai = dt * bf2f(aim);
  float br = dt * bf2f(bre), bi = dt * bf2f(bim);
  float cr = bf2f(cre), ci = bf2f(cim);
  float wr = cr * br - ci * bi, wi = cr * bi + ci * br;
  float ar2 = ar * 1.4426950408889634f, ai2 = ai * 0.15915494309189535f;
  f32x2 z4 = cexp2(f32x2{ar2 * 4.f, ai2 * 4.f}), z128 = cexp2(f32x2{ar2 * 128.f, ai2 * 128.f});
  KParams k;
  k.p0 = f32x4{ar2, ai2, wr, -wi};
  k.p1 = f32x4{z4.x, -z4.y, z128.x, z128.y};
  return k;
}

__global__ __launch_bounds__(512, 2) void k_l1(const u16* __restrict__ x, const u16* __restrict__ wg, const u16* __restrict__ wu,
                                               const u16* __restrict__ wd, const u16* __restrict__ Are, const u16* __restrict__ Aim,
                                               const u16* __restrict__ Bre, const u16* __restrict__ Bim, const u16* __restrict__ Cre,
                                               const u16* __restrict__ Cim, const u16* __restrict__ ldt, char* __restrict__ ws) {
  extern __shared__ __attribute__((aligned(16))) char sm[];
  const int tid = threadIdx.x, lane = tid & 63;
  const int wave = __builtin_amdgcn_readfirstlane(tid >> 6);
  const int b = blockIdx.x, xcd = b & 7, idx = b >> 3;
  const int mt = (xcd & 3) * 4 + (idx & 3), nt = (xcd >> 2) * 7 + (idx >> 2);
  u16* hfrag = (u16*)(ws + WS_H);
  u16* wdf = (u16*)(ws + WS_WD);
  u16* xnT = (u16*)(ws + WS_XT);
  u16* Kg = (u16*)(ws + WS_K);
  RSTAMP(0, 15); STAMP(0, 0); RSTAMP(3, 15); STAMP(3, 0);

  if (b == 0 && tid < 7) *(unsigned*)(ws + WS_CNT + tid * 256) = 0u;
  if (tid < 4) *(unsigned*)(sm + 896 + tid * 4) = 0u;

  const int sub = lane & 7;
  u32x4 raw[2][7];
#pragma unroll
  for (int p = 0; p < 2; ++p) {
    const int row = 16 * wave + 8 * p + (lane >> 3);
    const u32x4* xp = (const u32x4*)(x + (size_t)(mt * 128 + row) * D_);
#pragma unroll
    for (int j = 0; j < 7; ++j) raw[p][j] = xp[sub + 8 * j];
  }

  const int hw = wave - 4;
  const int dch = 2 * b + ((hw >> 1) & 1), hf = hw & 1;
  u16 pin[2][6];
  unsigned rt[2][8];
  u32x4 tapo[2];
  float dt = 0.f;
  char* kreg = sm + G1_K_OFF + ((hw >> 1) & 1) * 7168;
  KParams kp1;
  if (wave >= 4) {
#pragma unroll
    for (int i = 0; i < 2; ++i) {
      const int n = dch * N_ + hf * 128 + i * 64 + lane;
      pin[i][0] = Are[n]; pin[i][1] = Aim[n]; pin[i][2] = Bre[n]; pin[i][3] = Bim[n]; pin[i][4] = Cre[n]; pin[i][5] = Cim[n];
    }
    dt = rbf(expf(bf2f(ldt[dch])));
#pragma unroll
    for (int i = 0; i < 2; ++i) {
      int fr = b * 7 + hw + 4 * i;
      if (hw + 4 * i >= 7) fr = b * 7;
      const int fnt = fr / 112, fks = fr % 112, h = lane >> 5;
#pragma unroll
      for (int j = 0; j < 8; ++j) rt[i][j] = wd[(size_t)(16 * fks + 8 * (j >> 2) + 4 * h + (j & 3)) * D_ + 32 * fnt + (lane & 31)];
    }
    const u32x4* tp = (const u32x4*)(g_taptab.v + hf * 1024 + lane * 16);
    tapo[0] = tp[0]; tapo[1] = tp[1];
    KParams k0 = kparams(dt, pin[0][0], pin[0][1], pin[0][2], pin[0][3], pin[0][4], pin[0][5]);
    kp1 = kparams(dt, pin[1][0], pin[1][1], pin[1][2], pin[1][3], pin[1][4], pin[1][5]);
    *(f32x4*)(kreg + hf * 2048 + lane * 32) = k0.p0;
    *(f32x4*)(kreg + hf * 2048 + lane * 32 + 16) = k0.p1;
  }

  const u16* srcg = wg + (size_t)(lane >> 2) * F_ + nt * 128 + (wave & 3) * 32 + (lane & 3) * 8;
  const u16* srcu = wu + (size_t)(lane >> 2) * F_ + nt * 128 + (wave & 3) * 32 + (lane & 3) * 8;
  const unsigned bdst = (unsigned)(size_t)(LAS char*)sm + G1_B_OFF + (wave & 3) * 8192;
  auto glds = [&](const u16* src, unsigned dst) {
    unsigned keep;
    asm volatile("s_mov_b32 %0, m0\n\ts_mov_b32 m0, %2\n\ts_nop 0\n\tglobal_load_lds_dwordx4 %1, off\n\ts_mov_b32 m0, %0"
                 : "=&s"(keep) : "v"(src), "s"(dst) : "memory");
  };
  auto stage_g = [&](int s) { glds(srcg + (size_t)s * 16 * F_, bdst + (s & 3) * 2048); };
  auto stage_u = [&](int s) { glds(srcu + (size_t)s * 16 * F_, bdst + (s & 3) * 2048 + 1024); };

#pragma unroll
  for (int p = 0; p < 2; ++p) {
    const int row = 16 * wave + 8 * p + (lane >> 3);
    f32x2 xf[28];
    f32x2 s2 = {0.f, 0.f};
#pragma unroll
    for (int j = 0; j < 7; ++j)
#pragma unroll
      for (int e = 0; e < 4; ++e) { unsigned u = raw[p][j][e]; f32x2 v = {lo16(u), hi16(u)}; xf[j * 4 + e] = v; s2 += v; }
    float mean = red8(s2.x + s2.y) / 448.0f;
    float m = rbf(mean);
    f32x2 q2 = {0.f, 0.f};
    f32x2 mm = {m, m};
#pragma unroll
    for (int i = 0; i < 28; ++i) { f32x2 d = xf[i] - mm; xf[i] = d; q2 += d * d; }
    float dl = mean - m;
    float var = red8(q2.x + q2.y) / 448.0f - dl * dl;
    var = fmaxf(var, 0.f);
    float sd = rbf(sqrtf(rbf(rbf(var) + 1e-5f)));
    float r = 1.0f / sd;
#pragma unroll
    for (int j = 0; j < 7; ++j) {
      u32x4 o;
#pragma unroll
      for (int e = 0; e < 4; ++e) o[e] = cvtpk(rbf(xf[j * 4 + e].x) * r, rbf(xf[j * 4 + e].y) * r);
      *(u32x4*)(sm + row * A_STRIDE + (sub + 8 * j) * 16) = o;
    }
    if (p == 0) {
      STAMP(0, 1);
      WAITV(0);
      if (wave < 4) {
#pragma unroll
        for (int s = 0; s < 4; ++s) { stage_g(s); stage_u(s); }
      }
      SB();
    }
  }
  STAMP(0, 2);
  __syncthreads();
  STAMP(0, 3); STAMP(3, 3);

  if (wave >= 4) {
    {
      const int ht = tid - 256;
#pragma unroll
      for (int rep = 0; rep < 2; ++rep) {
        const int id = ht + 256 * rep;
        const int c = id >> 4, tq = id & 15;
        unsigned v[8];
#pragma unroll
        for (int i = 0; i < 8; ++i) v[i] = *(const u16*)(sm + (8 * tq + i) * A_STRIDE + (32 * nt + c) * 2);
        u32x4 o = {v[0] | (v[1] << 16), v[2] | (v[3] << 16), v[4] | (v[5] << 16), v[6] | (v[7] << 16)};
        *(u32x4*)(xnT + (size_t)(32 * nt + c) * L_ + mt * 128 + 8 * tq) = o;
      }
#pragma unroll
      for (int i = 0; i < 2; ++i) {
        if (hw + 4 * i < 7) {
          u32x4 o = {rt[i][0] | (rt[i][1] << 16), rt[i][2] | (rt[i][3] << 16), rt[i][4] | (rt[i][5] << 16), rt[i][6] | (rt[i][7] << 16)};
          *(u32x4*)((char*)wdf + ((size_t)(b * 7 + hw + 4 * i) * 64 + lane) * 16) = o;
        }
      }
    }
    STAMP(3, 4);
    const int row = lane & 15, g = lane >> 4;
    float tauA;
    {
      int t = row < 4 ? row : (row == 4 ? 256 : (row == 5 ? 258 : (row <= 12 ? (row - 4) * 256 : 0)));
      tauA = (float)t;
    }
    const f32x2 tA = {tauA, -tauA};
    const f32x2 tB = {(float)(8 * row), (float)(8 * row)};
    f32x4 acc[2][2];
#pragma unroll
    for (int a = 0; a < 2; ++a)
#pragma unroll
      for (int bb = 0; bb < 2; ++bb) acc[a][bb] = f32x4{0.f, 0.f, 0.f, 0.f};
    char* pbase = kreg + hf * 2048;
#pragma unroll
    for (int ks = 0; ks < 8; ++ks) {
      if (ks == 4) {
        __builtin_amdgcn_wave_barrier();
        *(f32x4*)(pbase + lane * 32) = kp1.p0;
        *(f32x4*)(pbase + lane * 32 + 16) = kp1.p1;
        __builtin_amdgcn_wave_barrier();
      }
      u32x4 Ah[2], Al[2], Bh[2], Bl[2];
#pragma unroll
      for (int i = 0; i < 4; ++i) {
        const int nl = 16 * (ks & 3) + 4 * g + i;
        f32x4 p0 = *(const f32x4*)(pbase + nl * 32), p1 = *(const f32x4*)(pbase + nl * 32 + 16);
        f32x2 aa = {p0.x, p0.y};
        f32x2 pa = cexp2(aa * tA);
        f32x2 u0 = cmul(f32x2{p0.z, p0.w}, pa);
        f32x2 u1 = cmul(u0, f32x2{p1.x, p1.y});
        f32x2 v0 = cexp2(aa * tB);
        f32x2 v1 = cmul(v0, f32x2{p1.z, p1.w});
        unsigned hh, ll;
        split2(u0, hh, ll); Ah[0][i] = hh; Al[0][i] = ll;
        split2(u1, hh, ll); Ah[1][i] = hh; Al[1][i] = ll;
        split2(v0, hh, ll); Bh[0][i] = hh; Bl[0][i] = ll;
        split2(v1, hh, ll); Bh[1][i] = hh; Bl[1][i] = ll;
      }
#pragma unroll
      for (int a = 0; a < 2; ++a)
#pragma unroll
        for (int bb = 0; bb < 2; ++bb) {
          acc[a][bb] = MFMA16(__builtin_bit_cast(bf16x8, Al[a]), __builtin_bit_cast(bf16x8, Bh[bb]), acc[a][bb]);
          acc[a][bb] = MFMA16(__builtin_bit_cast(bf16x8, Ah[a]), __builtin_bit_cast(bf16x8, Bl[bb]), acc[a][bb]);
          acc[a][bb] = MFMA16(__builtin_bit_cast(bf16x8, Ah[a]), __builtin_bit_cast(bf16x8, Bh[bb]), acc[a][bb]);
        }
    }
    STAMP(3, 5);
    char* tab = kreg + 4096;
    volatile LAS unsigned* flag = (volatile LAS unsigned*)(LAS char*)(sm + 896 + ((hw >> 1) & 1) * 8);
    if (hf == 1) {
      *(f32x4*)(tab + lane * 16) = acc[0][0];
      *(f32x4*)(tab + 1024 + lane * 16) = acc[0][1];
      if (lane < 32) { *(f32x4*)(tab + 2048 + lane * 16) = acc[1][0]; *(f32x4*)(tab + 2560 + lane * 16) = acc[1][1]; }
      WAITL(0);
      flag[0] = 1u;
      while (flag[1] == 0u) __builtin_amdgcn_s_sleep(1);
    } else {
      while (flag[0] == 0u) __builtin_amdgcn_s_sleep(1);
      asm volatile("" ::: "memory");
      f32x4 t0 = *(const f32x4*)(tab + lane * 16), t1 = *(const f32x4*)(tab + 1024 + lane * 16);
      *(f32x4*)(tab + lane * 16) = acc[0][0] + t0;
      *(f32x4*)(tab + 1024 + lane * 16) = acc[0][1] + t1;
      if (lane < 32) {
        f32x4 t2 = *(const f32x4*)(tab + 2048 + lane * 16), t3 = *(const f32x4*)(tab + 2560 + lane * 16);
        *(f32x4*)(tab + 2048 + lane * 16) = acc[1][0] + t2;
        *(f32x4*)(tab + 2560 + lane * 16) = acc[1][1] + t3;
      }
      WAITL(0);
      flag[1] = 1u;
    }
    asm volatile("" ::: "memory");
    STAMP(3, 6);
    u16* kh = Kg + (size_t)dch * 2 * KL;
#pragma unroll
    for (int c = 0; c < 2; ++c) {
      u32x4 oh, ol;
#pragma unroll
      for (int e = 0; e < 4; ++e) {
        const unsigned to = tapo[c][e];
        float k0 = *(const float*)(tab + (to & 0xffffu)), k1 = *(const float*)(tab + (to >> 16));
        unsigned hh, ll;
        split2(f32x2{k0, k1}, hh, ll);
        oh[e] = hh; ol[e] = ll;
      }
      *(u32x4*)(kh + hf * 1024 + lane * 16 + c * 8) = oh;
      *(u32x4*)(kh + KL + hf * 1024 + lane * 16 + c * 8) = ol;
    }
    if (lane < 8) { u32x4 z = {0, 0, 0, 0}; *(u32x4*)(kh + hf * KL + 2048 + 8 * lane) = z; }
    STAMP(3, 7); RSTAMP(3, 14);
    return;
  }

  const int h = lane >> 5, blk = (lane >> 4) & 1, q = (lane & 15) >> 2, p = lane & 3;
  const unsigned tra = G1_B_OFF + wave * 8192 + 64 * (8 * h + q) + 32 * blk + 8 * p;
  const unsigned xa0 = (lane & 31) * A_STRIDE + 16 * h;
  f32x16 acc[4][2];
#pragma unroll
  for (int a = 0; a < 4; ++a)
#pragma unroll
    for (int g = 0; g < 2; ++g)
#pragma unroll
      for (int i = 0; i < 16; ++i) acc[a][g][i] = 0.f;
  bf16x8 xf[2][4], wf_[2][2];
  auto rdx = [&](int s, int buf, int tt) { xf[buf][tt] = *(const bf16x8*)(sm + xa0 + tt * 32 * A_STRIDE + s * 32); };
  auto rdw = [&](int s, int buf, int g) {
    s16x4 a0 = __builtin_amdgcn_ds_read_tr16_b64_v4i16((LAS s16x4*)(sm + tra + (s & 3) * 2048 + g * 1024));
    s16x4 a1 = __builtin_amdgcn_ds_read_tr16_b64_v4i16((LAS s16x4*)(sm + tra + (s & 3) * 2048 + g * 1024 + 256));
    wf_[buf][g] = __builtin_shufflevector(a0, a1, 0, 1, 2, 3, 4, 5, 6, 7);
  };
#define MM(tt, g) acc[tt][g] = MFMA32(wf_[s & 1][g], xf[s & 1][tt], acc[tt][g])
  WAITV(6);
  rdw(0, 0, 0); rdw(0, 0, 1); rdx(0, 0, 0); rdx(0, 0, 1); rdx(0, 0, 2); rdx(0, 0, 3);
  SB();
#pragma unroll
  for (int s = 0; s < 28; ++s) {
    const int nb = (s + 1) & 1;
    MM(0, 0); SB();
    MM(0, 1); SB();
    if (s + 4 <= 27) stage_g(s + 4);
    SB();
    MM(1, 0); SB();
    if (s + 4 <= 27) stage_u(s + 4);
    SB();
    MM(1, 1); SB();
    if (s <= 26) {
      if (s <= 23) WAITV(6); else if (s == 24) WAITV(4); else if (s == 25) WAITV(2); else WAITV(0);
      rdw(s + 1, nb, 0); rdw(s + 1, nb, 1);
    }
    SB();
    MM(2, 0); SB();
    if (s <= 26) { rdx(s + 1, nb, 0); rdx(s + 1, nb, 1); }
    SB();
    MM(2, 1); SB();
    if (s <= 26) { rdx(s + 1, nb, 2); rdx(s + 1, nb, 3); }
    SB();
    MM(3, 0); SB();
    MM(3, 1); SB();
  }
  STAMP(0, 4);
#pragma unroll
  for (int tt = 0; tt < 4; ++tt)
#pragma unroll
    for (int s = 0; s < 2; ++s) {
      u32x4 o;
#pragma unroll
      for (int e = 0; e < 4; ++e)
        o[e] = cvtpk(rbf(acc[tt][0][8 * s + 2 * e]) * rbf(acc[tt][1][8 * s + 2 * e]), rbf(acc[tt][0][8 * s + 2 * e + 1]) * rbf(acc[tt][1][8 * s + 2 * e + 1]));
      const int mtile = 4 * mt + tt, kstep = 8 * nt + 2 * wave + s;
      *(u32x4*)((char*)hfrag + ((size_t)(mtile * 112 + kstep) * 64 + lane) * 16) = o;
    }
  STAMP(0, 5); RSTAMP(0, 14);
}

constexpr int CS = 4160;
constexpr int CV_X_OFF = 8 * CS;
constexpr int L2_LDS = 49152;

__global__ __launch_bounds__(256, 3) void k_l2(const u16* __restrict__ x, const u16* __restrict__ Dskip, char* __restrict__ ws,
                                               u16* __restrict__ out) {
  extern __shared__ __attribute__((aligned(16))) char sm[];
  const int tid = threadIdx.x, lane = tid & 63;
  const int wave = __builtin_amdgcn_readfirstlane(tid >> 6);
  u16* aT = (u16*)(ws + WS_AT);
  if (blockIdx.x >= 224) {
    const int d = blockIdx.x - 224;
    const u16* Kg = (const u16*)(ws + WS_K);
    const u16* xnT = (const u16*)(ws + WS_XT);
    RSTAMP(1, 15); STAMP(1, 0);
    {
      const char* src = (const char*)(Kg + (size_t)d * 2 * KL) + 2 * wave + lane * 16;
#pragma unroll
      for (int hl = 0; hl < 2; ++hl) {
#pragma unroll
        for (int j = 0; j < 4; ++j)
          __builtin_amdgcn_global_load_lds((const unsigned*)(src + hl * (KL * 2) + j * 1024), (unsigned*)(sm + (hl * 4 + wave) * CS + j * 1024), 16, 0, 0);
        if (lane < 4)
          __builtin_amdgcn_global_load_lds((const unsigned*)(src + hl * (KL * 2) + 4096), (unsigned*)(sm + (hl * 4 + wave) * CS + 4096), 16, 0, 0);
      }
      u32x4 xv = *(const u32x4*)(xnT + (size_t)d * L_ + tid * 8);
      *(u32x4*)(sm + CV_X_OFF + (32 + (tid >> 2)) * 80 + (tid & 3) * 16) = xv;
      if (tid < 160) { u32x4 z = {0, 0, 0, 0}; *(u32x4*)(sm + CV_X_OFF + tid * 16) = z; }
    }
    const float ds = bf2f(Dskip[d]);
    WAITV(0);
    __syncthreads();
    STAMP(1, 1);
    const int r = lane & 31, h = lane >> 5;
    const int cls = (3 - r) & 3;
    const unsigned abase = cls * CS + 2 * (2047 - 32 * (60 + wave) - r + 8 * h - cls);
    const unsigned bbase = CV_X_OFF + (64 + r - (60 + wave)) * 80 + 16 * h;
    f32x16 acc0, acc1;
#pragma unroll
    for (int i = 0; i < 16; ++i) { acc0[i] = 0.f; acc1[i] = 0.f; }
    bf16x8 Ah[2], Al[2], B1[2], B0[2];
    auto rd = [&](int st, int buf) {
      const int q = st >> 1, kk = st & 1;
      const unsigned ao = abase + 256 * (15 - q) + 32 * kk;
      s16x4 h0 = *(const volatile LAS s16x4*)(LAS char*)(sm + ao), h1 = *(const volatile LAS s16x4*)(LAS char*)(sm + ao + 8);
      s16x4 l0 = *(const volatile LAS s16x4*)(LAS char*)(sm + ao + 4 * CS), l1 = *(const volatile LAS s16x4*)(LAS char*)(sm + ao + 4 * CS + 8);
      Ah[buf] = __builtin_shufflevector(h0, h1, 0, 1, 2, 3, 4, 5, 6, 7);
      Al[buf] = __builtin_shufflevector(l0, l1, 0, 1, 2, 3, 4, 5, 6, 7);
      const unsigned bo = bbase + 320 * (15 - q) + 32 * kk;
      B1[buf] = *(const bf16x8*)(sm + bo);
      if (q < 8) B0[buf] = *(const bf16x8*)(sm + bo - 32 * 80);
    };
    rd(0, 0);
    SB();
#pragma unroll
    for (int st = 0; st < 32; ++st) {
      const int cb = st & 1;
      acc1 = MFMA32(Ah[cb], B1[cb], acc1);
      SB();
      if (st + 1 < 32) rd(st + 1, cb ^ 1);
      SB();
      acc1 = MFMA32(Al[cb], B1[cb], acc1);
      if (st < 16) {
        acc0 = MFMA32(Ah[cb], B0[cb], acc0);
        acc0 = MFMA32(Al[cb], B0[cb], acc0);
      }
      SB();
    }
    STAMP(1, 2);
    __syncthreads();
#pragma unroll
    for (int g = 0; g < 4; ++g) {
      *(f32x4*)(sm + wave * 8192 + g * 1024 + lane * 16) = f32x4{acc0[4 * g], acc0[4 * g + 1], acc0[4 * g + 2], acc0[4 * g + 3]};
      *(f32x4*)(sm + wave * 8192 + 4096 + g * 1024 + lane * 16) = f32x4{acc1[4 * g], acc1[4 * g + 1], acc1[4 * g + 2], acc1[4 * g + 3]};
    }
    __syncthreads();
    {
      const int g = tid >> 6, i = lane, tile = i >> 5, col = i & 31;
      f32x4 s0 = {0.f, 0.f, 0.f, 0.f}, s1 = {0.f, 0.f, 0.f, 0.f};
#pragma unroll
      for (int w = 0; w < 4; ++w) {
        s0 += *(const f32x4*)(sm + w * 8192 + tile * 4096 + g * 1024 + col * 16);
        s1 += *(const f32x4*)(sm + w * 8192 + tile * 4096 + g * 1024 + (col + 32) * 16);
      }
      u32x4 xv = *(const u32x4*)(sm + CV_X_OFF + (32 + i) * 80 + g * 16);
      float y[8] = {s0.x, s0.y, s0.z, s0.w, s1.x, s1.y, s1.z, s1.w};
      u32x4 o;
#pragma unroll
      for (int e = 0; e < 4; ++e)
        o[e] = cvtpk(rbf(y[2 * e]) + rbf(lo16(xv[e]) * ds), rbf(y[2 * e + 1]) + rbf(hi16(xv[e]) * ds));
      __syncthreads();
      *(u32x4*)(sm + (i * 4 + g) * 16) = o;
    }
    __syncthreads();
    {
      u32x4 o = *(const u32x4*)(sm + tid * 16);
      u16* dst = aT + (size_t)d * L_ + tid * 8;
      asm volatile("global_store_dwordx4 %0, %1, off sc1\n\ts_nop 1" ::"v"(dst), "v"(o) : "memory");
    }
    WAITV(0);
    __syncthreads();
    if (tid == 0) __hip_atomic_fetch_add((unsigned*)(ws + WS_CNT + (d >> 6) * 256), 1u, __ATOMIC_RELAXED, __HIP_MEMORY_SCOPE_AGENT);
    STAMP(1, 3); RSTAMP(1, 14);
    return;
  }
  const u16* hfrag = (const u16*)(ws + WS_H);
  const u16* wdf = (const u16*)(ws + WS_WD);
  const int b = blockIdx.x, xcd = b & 7, idx = b >> 3;
  const int mt2 = xcd * 4 + (idx & 3), nt2 = idx >> 2;
  const u32x4* hp = (const u32x4*)((const char*)hfrag + ((size_t)(2 * mt2) * 112 + 28 * wave) * 1024) + lane;
  const u32x4* wp = (const u32x4*)((const char*)wdf + ((size_t)(2 * nt2) * 112 + 28 * wave) * 1024) + lane;
  f32x16 acc[2][2];
#pragma unroll
  for (int i = 0; i < 2; ++i)
#pragma unroll
    for (int j = 0; j < 2; ++j)
#pragma unroll
      for (int e = 0; e < 16; ++e) acc[i][j][e] = 0.f;
  RSTAMP(2, 15); STAMP(2, 0);
  const int t = 64 * mt2 + 32 * (wave & 1) + (lane & 31), h = lane >> 5;
  const int d0 = 64 * nt2 + 32 * (wave >> 1) + 4 * h;
#define G2DP 4
  constexpr int DP = G2DP;
  u32x4 hb[DP][2], wb[DP][2];
#pragma unroll
  for (int ks = 0; ks < DP; ++ks) {
    hb[ks][0] = hp[ks * 64]; wb[ks][0] = wp[ks * 64]; wb[ks][1] = wp[(112 + ks) * 64]; hb[ks][1] = hp[(112 + ks) * 64];
  }
  SB();
  u32x2 xv[4];
#pragma unroll
  for (int ks = 0; ks < 28; ++ks) {
    const int sl = ks % DP;
    acc[0][0] = MFMA32(__builtin_bit_cast(bf16x8, wb[sl][0]), __builtin_bit_cast(bf16x8, hb[sl][0]), acc[0][0]);
    acc[0][1] = MFMA32(__builtin_bit_cast(bf16x8, wb[sl][1]), __builtin_bit_cast(bf16x8, hb[sl][0]), acc[0][1]);
    acc[1][0] = MFMA32(__builtin_bit_cast(bf16x8, wb[sl][0]), __builtin_bit_cast(bf16x8, hb[sl][1]), acc[1][0]);
    acc[1][1] = MFMA32(__builtin_bit_cast(bf16x8, wb[sl][1]), __builtin_bit_cast(bf16x8, hb[sl][1]), acc[1][1]);
    SB();
    if (ks + DP < 28) {
      hb[sl][0] = hp[(ks + DP) * 64]; wb[sl][0] = wp[(ks + DP) * 64]; wb[sl][1] = wp[(112 + ks + DP) * 64]; hb[sl][1] = hp[(112 + ks + DP) * 64];
    } else if (ks + DP == 28) {
#pragma unroll
      for (int g = 0; g < 4; ++g) xv[g] = *(const u32x2*)(x + (size_t)t * D_ + d0 + 8 * g);
    }
    SB();
  }
  STAMP(2, 1);
#pragma unroll
  for (int tl = 0; tl < 4; ++tl) {
    if (tl != wave) {
      const int slot = wave < tl ? wave : wave - 1;
#pragma unroll
      for (int g = 0; g < 4; ++g)
        *(f32x4*)(sm + (tl * 3 + slot) * 4096 + g * 1024 + lane * 16) =
            f32x4{acc[tl & 1][tl >> 1][4 * g], acc[tl & 1][tl >> 1][4 * g + 1], acc[tl & 1][tl >> 1][4 * g + 2], acc[tl & 1][tl >> 1][4 * g + 3]};
    }
  }
  if (wave == 0) {
    const unsigned* cnt = (const unsigned*)(ws + WS_CNT + nt2 * 256);
    for (unsigned spins = 0; spins < (1u << 22); ++spins) {
      unsigned c = __builtin_amdgcn_readfirstlane(__hip_atomic_load(cnt, __ATOMIC_RELAXED, __HIP_MEMORY_SCOPE_AGENT));
      if (c >= 64u) break;
      __builtin_amdgcn_s_sleep(2);
    }
  }
  __syncthreads();
  STAMP(2, 2);
  u16 av[16];
#pragma unroll
  for (int e = 0; e < 16; ++e)
    av[e] = __hip_atomic_load(aT + (size_t)(d0 + 8 * (e >> 2) + (e & 3)) * L_ + t, __ATOMIC_RELAXED, __HIP_MEMORY_SCOPE_AGENT);
  f32x16 own;
  if (wave == 0) own = acc[0][0]; else if (wave == 1) own = acc[1][0]; else if (wave == 2) own = acc[0][1]; else own = acc[1][1];
  float sum[16];
#pragma unroll
  for (int e = 0; e < 16; ++e) sum[e] = 0.f;
#pragma unroll
  for (int sw = 0; sw < 4; ++sw) {
    if (sw == wave) {
#pragma unroll
      for (int e = 0; e < 16; ++e) sum[e] += own[e];
    } else {
      const int slot = sw < wave ? sw : sw - 1;
#pragma unroll
      for (int g = 0; g < 4; ++g) {
        f32x4 v = *(const f32x4*)(sm + (wave * 3 + slot) * 4096 + g * 1024 + lane * 16);
        sum[4 * g] += v.x; sum[4 * g + 1] += v.y; sum[4 * g + 2] += v.z; sum[4 * g + 3] += v.w;
      }
    }
  }
#pragma unroll
  for (int g = 0; g < 4; ++g) {
    float o0 = rbf(lo16(xv[g].x) + bf2f(av[4 * g])) + rbf(sum[4 * g]);
    float o1 = rbf(hi16(xv[g].x) + bf2f(av[4 * g + 1])) + rbf(sum[4 * g + 1]);
    float o2 = rbf(lo16(xv[g].y) + bf2f(av[4 * g + 2])) + rbf(sum[4 * g + 2]);
    float o3 = rbf(hi16(xv[g].y) + bf2f(av[4 * g + 3])) + rbf(sum[4 * g + 3]);
    u32x2 o = {cvtpk(o0, o1), cvtpk(o2, o3)};
    *(u32x2*)(out + (size_t)t * D_ + d0 + 8 * g) = o;
  }
  STAMP(2, 3); RSTAMP(2, 14);
}

extern "C" void kernel_launch(void* const* d_in, const int* in_sizes, int n_in, void* d_out, int out_size, void* d_ws, size_t ws_size,
                              hipStream_t stream) {
  const u16* x = (const u16*)d_in[0];
  char* ws = (char*)d_ws;
  (void)hipFuncSetAttribute((const void*)k_l1, hipFuncAttributeMaxDynamicSharedMemorySize, G1_LDS);
  k_l1<<<224, 512, G1_LDS, stream>>>(x, (const u16*)d_in[10], (const u16*)d_in[11], (const u16*)d_in[12], (const u16*)d_in[1],
                                     (const u16*)d_in[2], (const u16*)d_in[3], (const u16*)d_in[4], (const u16*)d_in[5],
                                     (const u16*)d_in[6], (const u16*)d_in[7], ws);
  k_l2<<<672, 256, L2_LDS, stream>>>(x, (const u16*)d_in[8], ws, (u16*)d_out);
}
```

```cpp
#include <hip/hip_runtime.h>
#include <stdint.h>

typedef unsigned short u16;
typedef short bf16x8 __attribute__((ext_vector_type(8)));
typedef short s16x4 __attribute__((ext_vector_type(4)));
typedef float f32x16 __attribute__((ext_vector_type(16)));
typedef float f32x4 __attribute__((ext_vector_type(4)));
typedef float f32x2 __attribute__((ext_vector_type(2)));
typedef unsigned u32x4 __attribute__((ext_vector_type(4)));
typedef unsigned u32x2 __attribute__((ext_vector_type(2)));
typedef __bf16 bf16x2v __attribute__((ext_vector_type(2)));
typedef _Float16 f16x2v __attribute__((ext_vector_type(2)));
typedef _Float16 f16x8 __attribute__((ext_vector_type(8)));
#define LAS __attribute__((address_space(3)))
#define DI __device__ __forceinline__

constexpr int L_ = 2048, D_ = 448, N_ = 256, F_ = 1792;
constexpr int KL = 2112;

DI float bf2f(u16 v) { return __uint_as_float(((unsigned)v) << 16); }
DI unsigned cvtpk(float a, float b) { f32x2 p = {a, b}; return __builtin_bit_cast(unsigned, __builtin_convertvector(p, bf16x2v)); }
DI unsigned cvtpkh(float a, float b) { f32x2 p = {a, b}; return __builtin_bit_cast(unsigned, __builtin_convertvector(p, f16x2v)); }
DI float rbf(float f) { return __uint_as_float(cvtpk(0.f, f)); }
DI float lo16(unsigned u) { return __uint_as_float(u << 16); }
DI float hi16(unsigned u) { return __uint_as_float(u & 0xffff0000u); }
#define DPPF(v, ctrl) __builtin_bit_cast(float, __builtin_amdgcn_mov_dpp(__builtin_bit_cast(int, (v)), (ctrl), 0xF, 0xF, true))
DI float red8(float v) { v += DPPF(v, 0xB1); v += DPPF(v, 0x4E); v += DPPF(v, 0x141); return v; }
#define MFMA32(a, b, c) __builtin_amdgcn_mfma_f32_32x32x16_bf16((a), (b), (c), 0, 0, 0)
#define MFMA16H(a, b, c) __builtin_amdgcn_mfma_f32_16x16x32_f16(__builtin_bit_cast(f16x8, (a)), __builtin_bit_cast(f16x8, (b)), (c), 0, 0, 0)
#define MFMA32H(a, b, c) __builtin_amdgcn_mfma_f32_32x32x16_f16(__builtin_bit_cast(f16x8, (a)), __builtin_bit_cast(f16x8, (b)), (c), 0, 0, 0)
#define WAITV(n) asm volatile("s_waitcnt vmcnt(" #n ")" ::: "memory")
#define WAITL(n) asm volatile("s_waitcnt lgkmcnt(" #n ")" ::: "memory")
#define SB() __builtin_amdgcn_sched_barrier(0)
#define BC8(x) __builtin_bit_cast(bf16x8, (x))

#define STAMP(k, i)
#define RSTAMP(k, i)

constexpr size_t WS_CNT = 0;
constexpr size_t WS_H = 4096;
constexpr size_t WS_WD = WS_H + (size_t)64 * 112 * 1024;
constexpr size_t WS_XT = WS_WD + (size_t)14 * 112 * 1024;
constexpr size_t WS_K = WS_XT + (size_t)D_ * L_ * 2;
constexpr size_t WS_AT = WS_K + (size_t)D_ * KL * 2;
constexpr size_t WS_KS = WS_AT + (size_t)D_ * L_ * 2;

struct TapTab { u16 v[2048]; };
constexpr TapTab make_taptab() {
  TapTab t{};
  for (int i = 0; i < 2048; ++i) {
    int l = 2047 - i;
    int sh = l < 256 ? 0 : (l < 512 ? 1 : (l < 1024 ? 2 : 3));
    int v = l;
    if (sh) {
      int q = l >> sh, r = l & ((1 << sh) - 1), hf = 1 << (sh - 1);
      if (r > hf || (r == hf && (q & 1))) q += 1;
      v = q << sh;
    }
    int cp = (v & 255) >> 3, a = (v >> 2) & 1, h8 = v >> 8;
    int row = h8 == 0 ? (v & 3) : (h8 == 1 ? 4 + ((v >> 1) & 1) : 4 + h8);
    int bb = cp >> 4, col = cp & 15, ln = col + 16 * (row >> 2);
    int off = (a == 0 ? bb * 1024 : 2048 + bb * 512) + ln * 16 + (row & 3) * 4;
    t.v[i] = (u16)off;
  }
  return t;
}
__device__ const TapTab g_taptab = make_taptab();

DI float smul(float a, float b) { float r; asm("v_mul_f32 %0, %1, %2" : "=v"(r) : "v"(a), "v"(b)); return r; }
DI float ssub(float a, float b) { float r; asm("v_sub_f32 %0, %1, %2" : "=v"(r) : "v"(a), "v"(b)); return r; }
DI float sfma(float a, float b, float c) { float r; asm("v_fma_f32 %0, %1, %2, %3" : "=v"(r) : "v"(a), "v"(b), "v"(c)); return r; }
DI float sfnma(float a, float b, float c) { float r; asm("v_fma_f32 %0, -%1, %2, %3" : "=v"(r) : "v"(a), "v"(b), "v"(c)); return r; }
DI f32x2 cexp2(float e, float ph) {
  float m = __builtin_amdgcn_exp2f(e);
  float fr = __builtin_amdgcn_fractf(ph);
  return f32x2{m * __builtin_amdgcn_cosf(fr), m * __builtin_amdgcn_sinf(fr)};
}

struct KIn { const u16 *Are, *Aim, *Bre, *Bim, *Cre, *Cim, *ldt; };
DI void flag_wait(volatile LAS unsigned* f) {
  while (*f == 0u) __builtin_amdgcn_s_sleep(1);
  asm volatile("" ::: "memory");
}
DI void flag_set(volatile LAS unsigned* f) {
  WAITL(0);
  *f = 1u;
}
DI void tab_store(char* t, int lane, const f32x4 (&acc)[2][2]) {
  *(f32x4*)(t + lane * 16) = acc[0][0];
  *(f32x4*)(t + 1024 + lane * 16) = acc[0][1];
  if (lane < 32) { *(f32x4*)(t + 2048 + lane * 16) = acc[1][0]; *(f32x4*)(t + 2560 + lane * 16) = acc[1][1]; }
}
DI void tab_add(const char* t, int lane, f32x4 (&acc)[2][2]) {
  acc[0][0] += *(const f32x4*)(t + lane * 16);
  acc[0][1] += *(const f32x4*)(t + 1024 + lane * 16);
  acc[1][0] += *(const f32x4*)(t + 2048 + (lane & 31) * 16);
  acc[1][1] += *(const f32x4*)(t + 2560 + (lane & 31) * 16);
}
DI float wave_max(float v) {
#define DPPM(ctrl) v = fmaxf(v, __builtin_bit_cast(float, __builtin_amdgcn_update_dpp(__builtin_bit_cast(int, v), __builtin_bit_cast(int, v), (ctrl), 0xF, 0xF, false)))
  DPPM(0xB1); DPPM(0x4E); DPPM(0x141); DPPM(0x140);
#undef DPPM
  float a = __builtin_bit_cast(float, __builtin_amdgcn_readlane(__builtin_bit_cast(int, v), 0));
  float b = __builtin_bit_cast(float, __builtin_amdgcn_readlane(__builtin_bit_cast(int, v), 16));
  float c = __builtin_bit_cast(float, __builtin_amdgcn_readlane(__builtin_bit_cast(int, v), 32));
  float d = __builtin_bit_cast(float, __builtin_amdgcn_readlane(__builtin_bit_cast(int, v), 48));
  return fmaxf(fmaxf(a, b), fmaxf(c, d));
}
struct KP { f32x4 p0, p1; };
DI KP kparams(float dt, float sc, u16 are, u16 aim, float wr, float wi) {
  float ar2 = dt * bf2f(are) * 1.4426950408889634f, ai2 = dt * bf2f(aim) * 0.15915494309189535f;
  f32x2 z4 = cexp2(ar2 * 4.f, ai2 * 4.f), z128 = cexp2(ar2 * 128.f, ai2 * 128.f);
  KP k;
  k.p0 = f32x4{ar2, ai2, wr * sc, -wi * sc};
  k.p1 = f32x4{z4.x, -z4.y, z128.x, z128.y};
  return k;
}

DI void kgen_steps(const char* pbase, int lane, f32x4 (&acc)[2][2]) {
  const int row = lane & 15, g = lane >> 4;
  const float tauA = (float)(row < 4 ? row : (row == 4 ? 256 : (row == 5 ? 258 : (row <= 12 ? (row - 4) * 256 : 0))));
  const float ntauA = -tauA, tauB = (float)(8 * row);
#pragma unroll 1
  for (int ks = 0; ks < 4; ++ks) {
    const char* pk = pbase + ks * 512 + g * 128;
    f32x4 P0[4], P1[4];
#pragma unroll
    for (int i = 0; i < 4; ++i) { P0[i] = *(const f32x4*)(pk + i * 32); P1[i] = *(const f32x4*)(pk + i * 32 + 16); }
    u32x4 A[2], B[2];
    float ma[4], mb[4], fa[4], fb[4], Tr[4][4], Ti[4][4], t1[4], t2[4];
#pragma unroll
    for (int i = 0; i < 4; ++i) { ma[i] = smul(P0[i].x, tauA); fa[i] = smul(P0[i].y, ntauA); mb[i] = smul(P0[i].x, tauB); fb[i] = smul(P0[i].y, tauB); }
    SB();
#pragma unroll
    for (int i = 0; i < 4; ++i) { fa[i] = __builtin_amdgcn_fractf(fa[i]); fb[i] = __builtin_amdgcn_fractf(fb[i]); }
#pragma unroll
    for (int i = 0; i < 4; ++i) { ma[i] = __builtin_amdgcn_exp2f(ma[i]); mb[i] = __builtin_amdgcn_exp2f(mb[i]); }
    SB();
#pragma unroll
    for (int i = 0; i < 4; ++i) {
      t1[i] = __builtin_amdgcn_cosf(fa[i]); t2[i] = __builtin_amdgcn_sinf(fa[i]);
      Tr[2][i] = __builtin_amdgcn_cosf(fb[i]); Ti[2][i] = __builtin_amdgcn_sinf(fb[i]);
    }
    SB();
#pragma unroll
    for (int i = 0; i < 4; ++i) { t1[i] = smul(t1[i], ma[i]); t2[i] = smul(t2[i], ma[i]); Tr[2][i] = smul(Tr[2][i], mb[i]); Ti[2][i] = smul(Ti[2][i], mb[i]); }
    SB();
#pragma unroll
    for (int i = 0; i < 4; ++i) { Tr[0][i] = smul(P0[i].z, t1[i]); Ti[0][i] = smul(P0[i].z, t2[i]); Tr[3][i] = smul(Tr[2][i], P1[i].z); Ti[3][i] = smul(Tr[2][i], P1[i].w); }
    SB();
#pragma unroll
    for (int i = 0; i < 4; ++i) {
      Tr[0][i] = sfnma(P0[i].w, t2[i], Tr[0][i]); Ti[0][i] = sfma(P0[i].w, t1[i], Ti[0][i]);
      Tr[3][i] = sfnma(Ti[2][i], P1[i].w, Tr[3][i]); Ti[3][i] = sfma(Ti[2][i], P1[i].z, Ti[3][i]);
    }
    SB();
#pragma unroll
    for (int i = 0; i < 4; ++i) { Tr[1][i] = smul(Tr[0][i], P1[i].x); Ti[1][i] = smul(Tr[0][i], P1[i].y); B[0][i] = cvtpkh(Tr[2][i], Ti[2][i]); B[1][i] = cvtpkh(Tr[3][i], Ti[3][i]); }
    SB();
#pragma unroll
    for (int i = 0; i < 4; ++i) { Tr[1][i] = sfnma(Ti[0][i], P1[i].y, Tr[1][i]); Ti[1][i] = sfma(Ti[0][i], P1[i].x, Ti[1][i]); A[0][i] = cvtpkh(Tr[0][i], Ti[0][i]); }
    SB();
    acc[0][0] = MFMA16H(A[0], B[0], acc[0][0]);
    acc[0][1] = MFMA16H(A[0], B[1], acc[0][1]);
#pragma unroll
    for (int i = 0; i < 4; ++i) A[1][i] = cvtpkh(Tr[1][i], Ti[1][i]);
    SB();
    acc[1][0] = MFMA16H(A[1], B[0], acc[1][0]);
    acc[1][1] = MFMA16H(A[1], B[1], acc[1][1]);
  }
}

constexpr int A_STRIDE = 912;
constexpr int G1_A_BYTES = 128 * A_STRIDE;
constexpr int G1_B_OFF = G1_A_BYTES;
constexpr int G1_K_OFF = G1_B_OFF + 32768;
constexpr int G1_LDS = G1_K_OFF + 14336;

__global__ __launch_bounds__(512, 2) void k_l1(const u16* __restrict__ x, const u16* __restrict__ wg, const u16* __restrict__ wu,
                                               const u16* __restrict__ wd, const u16* __restrict__ Are, const u16* __restrict__ Aim,
                                               const u16* __restrict__ Bre, const u16* __restrict__ Bim, const u16* __restrict__ Cre,
                                               const u16* __restrict__ Cim, const u16* __restrict__ ldt, char* __restrict__ ws) {
  extern __shared__ __attribute__((aligned(16))) char sm[];
  const int tid = threadIdx.x, lane = tid & 63;
  const int wave = __builtin_amdgcn_readfirstlane(tid >> 6);
  const int b = blockIdx.x;
  u16* Kg = (u16*)(ws + WS_K);
  const int wq = wave & 3;

  const int xcd = b & 7, idx = b >> 3;
  const int mt = (xcd & 3) * 4 + (idx & 3), nt = (xcd >> 2) * 7 + (idx >> 2);
  u16* hfrag = (u16*)(ws + WS_H);
  u16* wdf = (u16*)(ws + WS_WD);
  u16* xnT = (u16*)(ws + WS_XT);
  RSTAMP(0, 15); STAMP(0, 0); RSTAMP(3, 15); STAMP(3, 0);

  if (b == 0 && tid < 7) *(unsigned*)(ws + WS_CNT + tid * 256) = 0u;
  if (tid < 4) *(unsigned*)(sm + 896 + tid * 4) = 0u;

  const int sub = lane & 7;
  u32x4 raw[2][7];
#pragma unroll
  for (int p = 0; p < 2; ++p) {
    const int row = 16 * wave + 8 * p + (lane >> 3);
    const u32x4* xp = (const u32x4*)(x + (size_t)(mt * 128 + row) * D_);
#pragma unroll
    for (int j = 0; j < 7; ++j) raw[p][j] = xp[sub + 8 * j];
  }

  const int dch = 2 * b + (wq >> 1), hf = wq & 1;
  char* kreg = sm + G1_K_OFF + (wq >> 1) * 7168;
  unsigned rt[2][8];
  u32x4 tapo[2];
  KP kp1;
  float kscale = 1.f;
  if (wave >= 4) {
    u16 pa[2][2], pb[4][4];
#pragma unroll
    for (int i = 0; i < 4; ++i) {
      const int n = dch * N_ + ((hf * 128 + i * 64 + lane) & 255);
      pb[i][0] = Bre[n]; pb[i][1] = Bim[n]; pb[i][2] = Cre[n]; pb[i][3] = Cim[n];
      if (i < 2) { pa[i][0] = Are[n]; pa[i][1] = Aim[n]; }
    }
    const float dt = rbf(expf(bf2f(ldt[dch])));
#pragma unroll
    for (int i = 0; i < 2; ++i) {
      int fr = b * 7 + wq + 4 * i;
      if (wq + 4 * i >= 7) fr = b * 7;
      const int fnt = fr / 112, fks = fr % 112, h = lane >> 5;
#pragma unroll
      for (int j = 0; j < 8; ++j) rt[i][j] = wd[(size_t)(16 * fks + 8 * (j >> 2) + 4 * h + (j & 3)) * D_ + 32 * fnt + (lane & 31)];
    }
    const u32x4* tp = (const u32x4*)(g_taptab.v + hf * 1024 + lane * 16);
    tapo[0] = tp[0]; tapo[1] = tp[1];
    float wr[4], wi[4], mx = 0.f;
#pragma unroll
    for (int i = 0; i < 4; ++i) {
      float br = dt * bf2f(pb[i][0]), bi = dt * bf2f(pb[i][1]), cr = bf2f(pb[i][2]), ci = bf2f(pb[i][3]);
      wr[i] = cr * br - ci * bi; wi[i] = cr * bi + ci * br;
      mx = fmaxf(mx, fmaxf(fabsf(wr[i]), fabsf(wi[i])));
    }
    mx = wave_max(mx);
    unsigned eb = (__float_as_uint(mx) >> 23) & 255u;
    eb = eb < 2u ? 126u : (eb > 250u ? 250u : eb);
    const float sc = __uint_as_float((253u - eb) << 23);
    kscale = __uint_as_float((eb + 1u) << 23);
    KP k0 = kparams(dt, sc, pa[0][0], pa[0][1], wr[0], wi[0]);
    kp1 = kparams(dt, sc, pa[1][0], pa[1][1], wr[1], wi[1]);
    *(f32x4*)(kreg + hf * 2048 + lane * 32) = k0.p0;
    *(f32x4*)(kreg + hf * 2048 + lane * 32 + 16) = k0.p1;
  }

  const u16* srcg = wg + (size_t)(lane >> 2) * F_ + nt * 128 + wq * 32 + (lane & 3) * 8;
  const u16* srcu = wu + (size_t)(lane >> 2) * F_ + nt * 128 + wq * 32 + (lane & 3) * 8;
  const unsigned bdst = (unsigned)(size_t)(LAS char*)sm + G1_B_OFF + wq * 8192;
  auto glds = [&](const u16* src, unsigned dst) {
    unsigned keep;
    asm volatile("s_mov_b32 %0, m0\n\ts_mov_b32 m0, %2\n\ts_nop 0\n\tglobal_load_lds_dwordx4 %1, off\n\ts_mov_b32 m0, %0"
                 : "=&s"(keep) : "v"(src), "s"(dst) : "memory");
  };

#pragma unroll
  for (int p = 0; p < 2; ++p) {
    const int row = 16 * wave + 8 * p + (lane >> 3);
    f32x2 xf[28];
    f32x2 s2 = {0.f, 0.f};
#pragma unroll
    for (int j = 0; j < 7; ++j)
#pragma unroll
      for (int e = 0; e < 4; ++e) { unsigned u = raw[p][j][e]; f32x2 v = {lo16(u), hi16(u)}; xf[j * 4 + e] = v; s2 += v; }
    float mean = red8(s2.x + s2.y) / 448.0f;
    float m = rbf(mean);
    f32x2 q2 = {0.f, 0.f};
    f32x2 mm = {m, m};
#pragma unroll
    for (int i = 0; i < 28; ++i) { f32x2 d = xf[i] - mm; xf[i] = d; q2 += d * d; }
    float dl = mean - m;
    float var = red8(q2.x + q2.y) / 448.0f - dl * dl;
    var = fmaxf(var, 0.f);
    float sd = rbf(sqrtf(rbf(rbf(var) + 1e-5f)));
    float r = 1.0f / sd;
#pragma unroll
    for (int j = 0; j < 7; ++j) {
      u32x4 o;
#pragma unroll
      for (int e = 0; e < 4; ++e) o[e] = cvtpk(rbf(xf[j * 4 + e].x) * r, rbf(xf[j * 4 + e].y) * r);
      *(u32x4*)(sm + row * A_STRIDE + (sub + 8 * j) * 16) = o;
    }
    if (p == 0) {
      STAMP(0, 1);
      WAITV(0);
      if (wave < 4) {
#pragma unroll
        for (int s = 0; s < 4; ++s) { glds(srcg + (size_t)s * 16 * F_, bdst + s * 2048); glds(srcu + (size_t)s * 16 * F_, bdst + s * 2048 + 1024); }
      }
      SB();
    }
  }
  STAMP(0, 2);
  __syncthreads();
  STAMP(0, 3); STAMP(3, 3);

  if (wave >= 4) {
    const int ht = tid - 256;
#pragma unroll
    for (int rep = 0; rep < 2; ++rep) {
      const int id = ht + 256 * rep;
      const int c = id >> 4, tq = id & 15;
      unsigned v[8];
#pragma unroll
      for (int i = 0; i < 8; ++i) v[i] = *(const u16*)(sm + (8 * tq + i) * A_STRIDE + (32 * nt + c) * 2);
      u32x4 o = {cvtpkh(lo16(v[0]), lo16(v[1])), cvtpkh(lo16(v[2]), lo16(v[3])), cvtpkh(lo16(v[4]), lo16(v[5])), cvtpkh(lo16(v[6]), lo16(v[7]))};
      *(u32x4*)(xnT + (size_t)(32 * nt + c) * L_ + mt * 128 + 8 * tq) = o;
    }
#pragma unroll
    for (int i = 0; i < 2; ++i) {
      if (wq + 4 * i < 7) {
        u32x4 o = {rt[i][0] | (rt[i][1] << 16), rt[i][2] | (rt[i][3] << 16), rt[i][4] | (rt[i][5] << 16), rt[i][6] | (rt[i][7] << 16)};
        *(u32x4*)((char*)wdf + ((size_t)(b * 7 + wq + 4 * i) * 64 + lane) * 16) = o;
      }
    }
    STAMP(3, 4);
    f32x4 kacc[2][2];
#pragma unroll
    for (int a = 0; a < 2; ++a)
#pragma unroll
      for (int bb = 0; bb < 2; ++bb) kacc[a][bb] = f32x4{0.f, 0.f, 0.f, 0.f};
    char* pbase = kreg + hf * 2048;
    kgen_steps(pbase, lane, kacc);
    __builtin_amdgcn_wave_barrier();
    *(f32x4*)(pbase + lane * 32) = kp1.p0;
    *(f32x4*)(pbase + lane * 32 + 16) = kp1.p1;
    __builtin_amdgcn_wave_barrier();
    kgen_steps(pbase, lane, kacc);
    STAMP(3, 5);
    char* tab = kreg + 4096;
    volatile LAS unsigned* flag = (volatile LAS unsigned*)(LAS char*)(sm + 896 + (wq >> 1) * 8);
    if (hf == 1) { tab_store(tab, lane, kacc); flag_set(flag); flag_wait(flag + 1); }
    else { flag_wait(flag); tab_add(tab, lane, kacc); tab_store(tab, lane, kacc); flag_set(flag + 1); }
    STAMP(3, 6);
    u16* kh = Kg + (size_t)dch * KL;
#pragma unroll
    for (int c = 0; c < 2; ++c) {
      u32x4 o;
#pragma unroll
      for (int e = 0; e < 4; ++e) {
        const unsigned to = tapo[c][e];
        o[e] = cvtpkh(*(const float*)(tab + (to & 0xffffu)), *(const float*)(tab + (to >> 16)));
      }
      *(u32x4*)(kh + hf * 1024 + lane * 16 + c * 8) = o;
    }
    if (hf == 0 && lane < 8) { u32x4 z = {0, 0, 0, 0}; *(u32x4*)(kh + 2048 + 8 * lane) = z; }
    if (hf == 1 && lane == 0) *(float*)(ws + WS_KS + dch * 4) = kscale;
    STAMP(3, 7); RSTAMP(3, 14);
    return;
  }

  const int h = lane >> 5, blk = (lane >> 4) & 1, q = (lane & 15) >> 2, p = lane & 3;
  const unsigned tra = G1_B_OFF + wave * 8192 + 64 * (8 * h + q) + 32 * blk + 8 * p;
  f32x16 acc[4][2];
#pragma unroll
  for (int a = 0; a < 4; ++a)
#pragma unroll
    for (int g = 0; g < 2; ++g)
#pragma unroll
      for (int i = 0; i < 16; ++i) acc[a][g][i] = 0.f;
  bf16x8 xf[2][4], wf_[2][2];
  unsigned xa = (lane & 31) * A_STRIDE + 16 * h;
  auto rdx = [&](int u, int buf, int tt) { xf[buf][tt] = *(const bf16x8*)(sm + xa + tt * 32 * A_STRIDE + u * 32); };
  auto rdw = [&](int u, int buf, int g) {
    s16x4 a0 = __builtin_amdgcn_ds_read_tr16_b64_v4i16((LAS s16x4*)(sm + tra + (u & 3) * 2048 + g * 1024));
    s16x4 a1 = __builtin_amdgcn_ds_read_tr16_b64_v4i16((LAS s16x4*)(sm + tra + (u & 3) * 2048 + g * 1024 + 256));
    wf_[buf][g] = __builtin_shufflevector(a0, a1, 0, 1, 2, 3, 4, 5, 6, 7);
  };
#define MM(tt, g) acc[tt][g] = MFMA32(wf_[u & 1][g], xf[u & 1][tt], acc[tt][g])
  WAITV(6);
  rdw(0, 0, 0); rdw(0, 0, 1); rdx(0, 0, 0); rdx(0, 0, 1); rdx(0, 0, 2); rdx(0, 0, 3);
  SB();
#pragma unroll 1
  for (int it = 0; it < 7; ++it) {
    const int nx = it < 6 ? it + 1 : 6;
    const u16* sg = srcg + (size_t)nx * 64 * F_;
    const u16* su = srcu + (size_t)nx * 64 * F_;
#pragma unroll
    for (int u = 0; u < 4; ++u) {
      const int nb = (u + 1) & 1;
      MM(0, 0); SB();
      MM(0, 1); SB();
      glds(sg + (size_t)u * 16 * F_, bdst + u * 2048);
      SB();
      MM(1, 0); SB();
      glds(su + (size_t)u * 16 * F_, bdst + u * 2048 + 1024);
      SB();
      MM(1, 1); SB();
      WAITV(6);
      rdw(u + 1, nb, 0); rdw(u + 1, nb, 1);
      SB();
      MM(2, 0); SB();
      rdx(u + 1, nb, 0); rdx(u + 1, nb, 1);
      SB();
      MM(2, 1); SB();
      rdx(u + 1, nb, 2); rdx(u + 1, nb, 3);
      SB();
      MM(3, 0); SB();
      MM(3, 1); SB();
    }
    xa += 128;
  }
  WAITV(0);
  STAMP(0, 4);
#pragma unroll
  for (int tt = 0; tt < 4; ++tt)
#pragma unroll
    for (int s = 0; s < 2; ++s) {
      u32x4 o;
#pragma unroll
      for (int e = 0; e < 4; ++e)
        o[e] = cvtpk(rbf(acc[tt][0][8 * s + 2 * e]) * rbf(acc[tt][1][8 * s + 2 * e]), rbf(acc[tt][0][8 * s + 2 * e + 1]) * rbf(acc[tt][1][8 * s + 2 * e + 1]));
      const int mtile = 4 * mt + tt, kstep = 8 * nt + 2 * wave + s;
      *(u32x4*)((char*)hfrag + ((size_t)(mtile * 112 + kstep) * 64 + lane) * 16) = o;
    }
  STAMP(0, 5); RSTAMP(0, 14);
}

constexpr int CS = 4160;
constexpr int CV_K_OFF = 512;
constexpr int CV_X_OFF = 33792;
constexpr int L2_LDS = 49152;
#define POLL_SLEEP 8

__global__ __launch_bounds__(256, 3) void k_l2(const u16* __restrict__ x, const u16* __restrict__ Dskip, char* __restrict__ ws,
                                               u16* __restrict__ out) {
  extern __shared__ __attribute__((aligned(16))) char sm[];
  const int tid = threadIdx.x, lane = tid & 63;
  const int wave = __builtin_amdgcn_readfirstlane(tid >> 6);
  u16* aT = (u16*)(ws + WS_AT);
  if (blockIdx.x < 448) {
    const int d = blockIdx.x;
    const u16* kh = (const u16*)(ws + WS_K) + (size_t)d * KL + tid * 8;
    const u16* xnT = (const u16*)(ws + WS_XT);
    RSTAMP(2, 15); STAMP(2, 0);
    {
      u32x4 ka = *(const u32x4*)(kh);
      u32x2 kb = *(const u32x2*)(kh + 8);
      u32x4 xv = *(const u32x4*)(xnT + (size_t)d * L_ + tid * 8);
      u32x4 z = {0, 0, 0, 0};
      if (tid < 160) *(u32x4*)(sm + CV_X_OFF + tid * 16) = z;
      if (tid < 16) *(u32x4*)(sm + CV_K_OFF + (tid >> 2) * CS + 4096 + (tid & 3) * 16) = z;
      const unsigned a10 = __builtin_amdgcn_alignbit(ka[1], ka[0], 16), a21 = __builtin_amdgcn_alignbit(ka[2], ka[1], 16);
      const unsigned a32 = __builtin_amdgcn_alignbit(ka[3], ka[2], 16), a43 = __builtin_amdgcn_alignbit(kb[0], ka[3], 16);
      const unsigned a54 = __builtin_amdgcn_alignbit(kb[1], kb[0], 16);
      char* dst = sm + CV_K_OFF + tid * 16;
      *(u32x4*)(dst) = ka;
      *(u32x4*)(dst + CS) = u32x4{a10, a21, a32, a43};
      *(u32x4*)(dst + 2 * CS) = u32x4{ka[1], ka[2], ka[3], kb[0]};
      *(u32x4*)(dst + 3 * CS) = u32x4{a21, a32, a43, a54};
      *(u32x4*)(sm + CV_X_OFF + (32 + (tid >> 2)) * 80 + (tid & 3) * 16) = xv;
    }
    const float ksc = *(const float*)(ws + WS_KS + d * 4);
    const float ds = bf2f(Dskip[d]);
    __syncthreads();
    STAMP(2, 1);
    const int r = lane & 31, h = lane >> 5;
    const int cls = (3 - r) & 3;
    unsigned ao = CV_K_OFF + cls * CS + 2 * (2047 - 32 * wave - r + 8 * h - cls) - 256;
    unsigned bo = CV_X_OFF + (32 + r - wave) * 80 + 16 * h - 320;
    f32x16 acc0, acc1;
#pragma unroll
    for (int i = 0; i < 16; ++i) { acc0[i] = 0.f; acc1[i] = 0.f; }
    bf16x8 Af[2], B1[2], B0[2];
    auto rd = [&](int nxt, int kk, int buf, bool both) {
      const unsigned a_ = ao + (nxt ? 0 : 256) + 32 * kk;
      s16x4 h0 = *(const volatile LAS s16x4*)(LAS char*)(sm + a_), h1 = *(const volatile LAS s16x4*)(LAS char*)(sm + a_ + 8);
      Af[buf] = __builtin_shufflevector(h0, h1, 0, 1, 2, 3, 4, 5, 6, 7);
      const unsigned b_ = bo + (nxt ? 0 : 320) + 32 * kk;
      B1[buf] = *(const bf16x8*)(sm + b_ + 2560);
      if (both) B0[buf] = *(const bf16x8*)(sm + b_);
    };
    rd(0, 0, 0, true);
    SB();
#pragma unroll 1
    for (int q = 0; q < 8; ++q) {
      rd(0, 1, 1, true); SB();
      acc1 = MFMA32H(Af[0], B1[0], acc1);
      acc0 = MFMA32H(Af[0], B0[0], acc0); SB();
      rd(1, 0, 0, true); SB();
      acc1 = MFMA32H(Af[1], B1[1], acc1);
      acc0 = MFMA32H(Af[1], B0[1], acc0); SB();
      ao -= 256; bo -= 320;
    }
#pragma unroll 1
    for (int q = 8; q < 16; ++q) {
      rd(0, 1, 1, false); SB();
      acc1 = MFMA32H(Af[0], B1[0], acc1); SB();
      rd(1, 0, 0, false); SB();
      acc1 = MFMA32H(Af[1], B1[1], acc1); SB();
      ao -= 256; bo -= 320;
    }
    STAMP(2, 2);
    __syncthreads();
#pragma unroll
    for (int g = 0; g < 4; ++g) {
      *(f32x4*)(sm + wave * 8192 + g * 1024 + lane * 16) = f32x4{acc0[4 * g], acc0[4 * g + 1], acc0[4 * g + 2], acc0[4 * g + 3]};
      *(f32x4*)(sm + wave * 8192 + 4096 + g * 1024 + lane * 16) = f32x4{acc1[4 * g], acc1[4 * g + 1], acc1[4 * g + 2], acc1[4 * g + 3]};
    }
    __syncthreads();
    {
      const int g = tid >> 6, i = lane, tile = i >> 5, col = i & 31;
      f32x4 s0 = {0.f, 0.f, 0.f, 0.f}, s1 = {0.f, 0.f, 0.f, 0.f};
#pragma unroll
      for (int w = 0; w < 4; ++w) {
        s0 += *(const f32x4*)(sm + w * 8192 + tile * 4096 + g * 1024 + col * 16);
        s1 += *(const f32x4*)(sm + w * 8192 + tile * 4096 + g * 1024 + (col + 32) * 16);
      }
      u32x4 xv = *(const u32x4*)(sm + CV_X_OFF + (32 + i) * 80 + g * 16);
      float y[8] = {s0.x, s0.y, s0.z, s0.w, s1.x, s1.y, s1.z, s1.w};
      u32x4 o;
#pragma unroll
      for (int e = 0; e < 4; ++e) {
        const unsigned xe = xv[e];
        f16x2v xh = __builtin_bit_cast(f16x2v, xe);
        o[e] = cvtpk(rbf(y[2 * e] * ksc) + rbf((float)xh.x * ds), rbf(y[2 * e + 1] * ksc) + rbf((float)xh.y * ds));
      }
      *(u32x4*)(sm + 41984 + (i * 4 + g) * 16) = o;
    }
    __syncthreads();
    {
      u32x4 o = *(const u32x4*)(sm + 41984 + tid * 16);
      u16* dst = aT + (size_t)d * L_ + tid * 8;
      asm volatile("global_store_dwordx4 %0, %1, off sc1\n\ts_nop 1" ::"v"(dst), "v"(o) : "memory");
    }
    WAITV(0);
    __syncthreads();
    if (tid == 0) __hip_atomic_fetch_add((unsigned*)(ws + WS_CNT + (d >> 6) * 256), 1u, __ATOMIC_RELAXED, __HIP_MEMORY_SCOPE_AGENT);
    STAMP(2, 3); RSTAMP(2, 14);
    return;
  }
  const u16* hfrag = (const u16*)(ws + WS_H);
  const u16* wdf = (const u16*)(ws + WS_WD);
  const int b = blockIdx.x - 448;
  const int xcd = b & 7, idx = b >> 3;
  const int mt2 = xcd * 4 + (idx & 3), nt2 = idx >> 2;
  const u32x4* hp = (const u32x4*)((const char*)hfrag + ((size_t)(2 * mt2) * 112 + 28 * wave) * 1024) + lane;
  const u32x4* wp = (const u32x4*)((const char*)wdf + ((size_t)(2 * nt2) * 112 + 28 * wave) * 1024) + lane;
  f32x16 acc[2][2];
#pragma unroll
  for (int i = 0; i < 2; ++i)
#pragma unroll
    for (int j = 0; j < 2; ++j)
#pragma unroll
      for (int e = 0; e < 16; ++e) acc[i][j][e] = 0.f;
  RSTAMP(4, 15); STAMP(4, 0);
  const int t = 64 * mt2 + 32 * (wave & 1) + (lane & 31), h = lane >> 5;
  const int d0 = 64 * nt2 + 32 * (wave >> 1) + 4 * h;
#define G2PRIO 0
  __builtin_amdgcn_s_setprio(G2PRIO);
#define G2DELAY 0
  if (G2DELAY) __builtin_amdgcn_s_sleep(G2DELAY);
  constexpr int DP = 4;
  u32x4 hb[DP][2], wb[DP][2];
#pragma unroll
  for (int ks = 0; ks < DP; ++ks) {
    hb[ks][0] = hp[ks * 64]; wb[ks][0] = wp[ks * 64]; wb[ks][1] = wp[(112 + ks) * 64]; hb[ks][1] = hp[(112 + ks) * 64];
  }
  u32x2 xv[4];
#pragma unroll
  for (int g = 0; g < 4; ++g) xv[g] = *(const u32x2*)(x + (size_t)t * D_ + d0 + 8 * g);
  SB();
#pragma unroll 1
  for (int it = 0; it < 6; ++it) {
    hp += DP * 64; wp += DP * 64;
#pragma unroll
    for (int sl = 0; sl < DP; ++sl) {
      acc[0][0] = MFMA32(BC8(wb[sl][0]), BC8(hb[sl][0]), acc[0][0]);
      acc[0][1] = MFMA32(BC8(wb[sl][1]), BC8(hb[sl][0]), acc[0][1]);
      acc[1][0] = MFMA32(BC8(wb[sl][0]), BC8(hb[sl][1]), acc[1][0]);
      acc[1][1] = MFMA32(BC8(wb[sl][1]), BC8(hb[sl][1]), acc[1][1]);
      SB();
      hb[sl][0] = hp[sl * 64]; wb[sl][0] = wp[sl * 64]; wb[sl][1] = wp[(112 + sl) * 64]; hb[sl][1] = hp[(112 + sl) * 64];
      SB();
    }
  }
#pragma unroll
  for (int sl = 0; sl < DP; ++sl) {
    acc[0][0] = MFMA32(BC8(wb[sl][0]), BC8(hb[sl][0]), acc[0][0]);
    acc[0][1] = MFMA32(BC8(wb[sl][1]), BC8(hb[sl][0]), acc[0][1]);
    acc[1][0] = MFMA32(BC8(wb[sl][0]), BC8(hb[sl][1]), acc[1][0]);
    acc[1][1] = MFMA32(BC8(wb[sl][1]), BC8(hb[sl][1]), acc[1][1]);
    SB();
  }
  STAMP(4, 1);
#pragma unroll
  for (int tl = 0; tl < 4; ++tl) {
    if (tl != wave) {
      const int slot = wave < tl ? wave : wave - 1;
#pragma unroll
      for (int g = 0; g < 4; ++g)
        *(f32x4*)(sm + (tl * 3 + slot) * 4096 + g * 1024 + lane * 16) =
            f32x4{acc[tl & 1][tl >> 1][4 * g], acc[tl & 1][tl >> 1][4 * g + 1], acc[tl & 1][tl >> 1][4 * g + 2], acc[tl & 1][tl >> 1][4 * g + 3]};
    }
  }
  if (wave == 0) {
    const unsigned* cnt = (const unsigned*)(ws + WS_CNT + nt2 * 256);
    for (unsigned spins = 0; spins < (1u << 20); ++spins) {
      unsigned c = __builtin_amdgcn_readfirstlane(__hip_atomic_load(cnt, __ATOMIC_RELAXED, __HIP_MEMORY_SCOPE_AGENT));
      if (c >= 64u) break;
      __builtin_amdgcn_s_sleep(POLL_SLEEP);
    }
  }
  __syncthreads();
  STAMP(4, 2);
  u16 av[16];
#pragma unroll
  for (int e = 0; e < 16; ++e)
    av[e] = __hip_atomic_load(aT + (size_t)(d0 + 8 * (e >> 2) + (e & 3)) * L_ + t, __ATOMIC_RELAXED, __HIP_MEMORY_SCOPE_AGENT);
  f32x16 own;
  if (wave == 0) own = acc[0][0]; else if (wave == 1) own = acc[1][0]; else if (wave == 2) own = acc[0][1]; else own = acc[1][1];
  float sum[16];
#pragma unroll
  for (int e = 0; e < 16; ++e) sum[e] = 0.f;
#pragma unroll
  for (int sw = 0; sw < 4; ++sw) {
    if (sw == wave) {
#pragma unroll
      for (int e = 0; e < 16; ++e) sum[e] += own[e];
    } else {
      const int slot = sw < wave ? sw : sw - 1;
#pragma unroll
      for (int g = 0; g < 4; ++g) {
        f32x4 v = *(const f32x4*)(sm + (wave * 3 + slot) * 4096 + g * 1024 + lane * 16);
        sum[4 * g] += v.x; sum[4 * g + 1] += v.y; sum[4 * g + 2] += v.z; sum[4 * g + 3] += v.w;
      }
    }
  }
#pragma unroll
  for (int g = 0; g < 4; ++g) {
    float o0 = rbf(lo16(xv[g].x) + bf2f(av[4 * g])) + rbf(sum[4 * g]);
    float o1 = rbf(hi16(xv[g].x) + bf2f(av[4 * g + 1])) + rbf(sum[4 * g + 1]);
    float o2 = rbf(lo16(xv[g].y) + bf2f(av[4 * g + 2])) + rbf(sum[4 * g + 2]);
    float o3 = rbf(hi16(xv[g].y) + bf2f(av[4 * g + 3])) + rbf(sum[4 * g + 3]);
    u32x2 o = {cvtpk(o0, o1), cvtpk(o2, o3)};
    *(u32x2*)(out + (size_t)t * D_ + d0 + 8 * g) = o;
  }
  STAMP(4, 3); RSTAMP(4, 14);
}

extern "C" void kernel_launch(void* const* d_in, const int* in_sizes, int n_in, void* d_out, int out_size, void* d_ws, size_t ws_size,
                              hipStream_t stream) {
  const u16* x = (const u16*)d_in[0];
  char* ws = (char*)d_ws;
  (void)hipFuncSetAttribute((const void*)k_l1, hipFuncAttributeMaxDynamicSharedMemorySize, G1_LDS);
  k_l1<<<224, 512, G1_LDS, stream>>>(x, (const u16*)d_in[10], (const u16*)d_in[11], (const u16*)d_in[12], (const u16*)d_in[1],
                                     (const u16*)d_in[2], (const u16*)d_in[3], (const u16*)d_in[4], (const u16*)d_in[5],
                                     (const u16*)d_in[6], (const u16*)d_in[7], ws);
  k_l2<<<672, 256, L2_LDS, stream>>>(x, (const u16*)d_in[8], ws, (u16*)d_out);
}
```

```cpp
#include <hip/hip_runtime.h>
#include <stdint.h>

#if defined(__HIP_DEVICE_COMPILE__)
#pragma clang attribute push(__attribute__((target("no-packed-fp32-ops"))), apply_to = function)
#endif
#define U2F(x) __builtin_bit_cast(float, (unsigned)(x))
#define F2U(x) __builtin_bit_cast(unsigned, (float)(x))
#define I2F(x) __builtin_bit_cast(float, (int)(x))
#define F2I(x) __builtin_bit_cast(int, (float)(x))
#define TIDX ((int)__builtin_amdgcn_workitem_id_x())
#define BIDX ((int)__builtin_amdgcn_workgroup_id_x())
#define SYNC() do { __builtin_amdgcn_fence(__ATOMIC_RELEASE, "workgroup"); __builtin_amdgcn_s_barrier(); __builtin_amdgcn_fence(__ATOMIC_ACQUIRE, "workgroup"); } while (0)

typedef unsigned short u16;
typedef short bf16x8 __attribute__((ext_vector_type(8)));
typedef short s16x4 __attribute__((ext_vector_type(4)));
typedef float f32x16 __attribute__((ext_vector_type(16)));
typedef float f32x4 __attribute__((ext_vector_type(4)));
typedef float f32x2 __attribute__((ext_vector_type(2)));
typedef unsigned u32x4 __attribute__((ext_vector_type(4)));
typedef unsigned u32x2 __attribute__((ext_vector_type(2)));
typedef __bf16 bf16x2v __attribute__((ext_vector_type(2)));
typedef _Float16 f16x2v __attribute__((ext_vector_type(2)));
typedef _Float16 f16x8 __attribute__((ext_vector_type(8)));
#define LAS __attribute__((address_space(3)))
#define DI __device__ __forceinline__

constexpr int L_ = 2048, D_ = 448, N_ = 256, F_ = 1792;
constexpr int KL = 2112;

DI float bf2f(u16 v) { return U2F(((unsigned)v) << 16); }
DI unsigned cvtpk(float a, float b) { f32x2 p = {a, b}; return __builtin_bit_cast(unsigned, __builtin_convertvector(p, bf16x2v)); }
DI unsigned cvtpkh(float a, float b) { f32x2 p = {a, b}; return __builtin_bit_cast(unsigned, __builtin_convertvector(p, f16x2v)); }
DI float exp_acc(float l) {
  const float t = l * 1.4426950408889634f;
  const float r = __builtin_fmaf(l, 1.4426950408889634f, -t) + l * 1.9259629911e-8f;
  const float e = __builtin_amdgcn_exp2f(t);
  return __builtin_fmaf(e, r * 0.6931471805599453f, e);
}
DI float rbf(float f) { return U2F(cvtpk(0.f, f)); }
DI float lo16(unsigned u) { return U2F(u << 16); }
DI float hi16(unsigned u) { return U2F(u & 0xffff0000u); }
#define DPPF(v, ctrl) __builtin_bit_cast(float, __builtin_amdgcn_mov_dpp(__builtin_bit_cast(int, (v)), (ctrl), 0xF, 0xF, true))
DI float red8(float v) { v += DPPF(v, 0xB1); v += DPPF(v, 0x4E); v += DPPF(v, 0x141); return v; }
#define MFMA32(a, b, c) __builtin_amdgcn_mfma_f32_32x32x16_bf16((a), (b), (c), 0, 0, 0)
#define MFMA16H(a, b, c) __builtin_amdgcn_mfma_f32_16x16x32_f16(__builtin_bit_cast(f16x8, (a)), __builtin_bit_cast(f16x8, (b)), (c), 0, 0, 0)
#define MFMA32H(a, b, c) __builtin_amdgcn_mfma_f32_32x32x16_f16(__builtin_bit_cast(f16x8, (a)), __builtin_bit_cast(f16x8, (b)), (c), 0, 0, 0)
#define WAITV(n) asm volatile("s_waitcnt vmcnt(" #n ")" ::: "memory")
#define WAITL(n) asm volatile("s_waitcnt lgkmcnt(" #n ")" ::: "memory")
#define SB() __builtin_amdgcn_sched_barrier(0)
#define BC8(x) __builtin_bit_cast(bf16x8, (x))

#define STAMP(k, i)
#define RSTAMP(k, i)

constexpr size_t WS_CNT = 0;
constexpr size_t WS_H = 4096;
constexpr size_t WS_WD = WS_H + (size_t)64 * 112 * 1024;
constexpr size_t WS_XT = WS_WD + (size_t)14 * 112 * 1024;
constexpr size_t WS_K = WS_XT + (size_t)D_ * L_ * 2;
constexpr size_t WS_AT = WS_K + (size_t)D_ * KL * 2;
constexpr size_t WS_KS = WS_AT + (size_t)D_ * L_ * 2;

struct TapTab { u16 v[2048]; };
constexpr TapTab make_taptab() {
  TapTab t{};
  for (int i = 0; i < 2048; ++i) {
    int l = 2047 - i;
    int sh = l < 256 ? 0 : (l < 512 ? 1 : (l < 1024 ? 2 : 3));
    int v = l;
    if (sh) {
      int q = l >> sh, r = l & ((1 << sh) - 1), hf = 1 << (sh - 1);
      if (r > hf || (r == hf && (q & 1))) q += 1;
      v = q << sh;
    }
    int cp = (v & 255) >> 3, a = (v >> 2) & 1, h8 = v >> 8;
    int row = h8 == 0 ? (v & 3) : (h8 == 1 ? 4 + ((v >> 1) & 1) : 4 + h8);
    int bb = cp >> 4, col = cp & 15, ln = col + 16 * (row >> 2);
    int off = (a == 0 ? bb * 1024 : 2048 + bb * 512) + ln * 16 + (row & 3) * 4;
    t.v[i] = (u16)off;
  }
  return t;
}
__device__ const TapTab g_taptab = make_taptab();

DI float smul(float a, float b) { float r; asm("v_mul_f32 %0, %1, %2" : "=v"(r) : "v"(a), "v"(b)); return r; }
DI float ssub(float a, float b) { float r; asm("v_sub_f32 %0, %1, %2" : "=v"(r) : "v"(a), "v"(b)); return r; }
DI float sfma(float a, float b, float c) { float r; asm("v_fma_f32 %0, %1, %2, %3" : "=v"(r) : "v"(a), "v"(b), "v"(c)); return r; }
DI float sfnma(float a, float b, float c) { float r; asm("v_fma_f32 %0, -%1, %2, %3" : "=v"(r) : "v"(a), "v"(b), "v"(c)); return r; }
DI f32x2 cexp2(float e, float ph) {
  float m = __builtin_amdgcn_exp2f(e);
  float fr = __builtin_amdgcn_fractf(ph);
  return f32x2{m * __builtin_amdgcn_cosf(fr), m * __builtin_amdgcn_sinf(fr)};
}

struct KIn { const u16 *Are, *Aim, *Bre, *Bim, *Cre, *Cim, *ldt; };
DI void flag_wait(volatile LAS unsigned* f) {
  while (*f == 0u) __builtin_amdgcn_s_sleep(1);
  asm volatile("" ::: "memory");
}
DI void flag_set(volatile LAS unsigned* f) {
  WAITL(0);
  *f = 1u;
}
DI void tab_store(char* t, int lane, const f32x4 (&acc)[2][2]) {
  *(f32x4*)(t + lane * 16) = acc[0][0];
  *(f32x4*)(t + 1024 + lane * 16) = acc[0][1];
  if (lane < 32) { *(f32x4*)(t + 2048 + lane * 16) = acc[1][0]; *(f32x4*)(t + 2560 + lane * 16) = acc[1][1]; }
}
DI void tab_add(const char* t, int lane, f32x4 (&acc)[2][2]) {
  acc[0][0] += *(const f32x4*)(t + lane * 16);
  acc[0][1] += *(const f32x4*)(t + 1024 + lane * 16);
  acc[1][0] += *(const f32x4*)(t + 2048 + (lane & 31) * 16);
  acc[1][1] += *(const f32x4*)(t + 2560 + (lane & 31) * 16);
}
DI float wave_max(float v) {
#define DPPM(ctrl) v = __builtin_fmaxf(v, __builtin_bit_cast(float, __builtin_amdgcn_update_dpp(__builtin_bit_cast(int, v), __builtin_bit_cast(int, v), (ctrl), 0xF, 0xF, false)))
  DPPM(0xB1); DPPM(0x4E); DPPM(0x141); DPPM(0x140);
#undef DPPM
  float a = __builtin_bit_cast(float, __builtin_amdgcn_readlane(__builtin_bit_cast(int, v), 0));
  float b = __builtin_bit_cast(float, __builtin_amdgcn_readlane(__builtin_bit_cast(int, v), 16));
  float c = __builtin_bit_cast(float, __builtin_amdgcn_readlane(__builtin_bit_cast(int, v), 32));
  float d = __builtin_bit_cast(float, __builtin_amdgcn_readlane(__builtin_bit_cast(int, v), 48));
  return __builtin_fmaxf(__builtin_fmaxf(a, b), __builtin_fmaxf(c, d));
}
struct KP { f32x4 p0, p1; };
DI KP kparams(float dt, float sc, u16 are, u16 aim, float wr, float wi) {
  float ar2 = dt * bf2f(are) * 1.4426950408889634f, ai2 = dt * bf2f(aim) * 0.15915494309189535f;
  f32x2 z4 = cexp2(ar2 * 4.f, ai2 * 4.f), z128 = cexp2(ar2 * 128.f, ai2 * 128.f);
  KP k;
  k.p0 = f32x4{ar2, ai2, wr * sc, -wi * sc};
  k.p1 = f32x4{z4.x, -z4.y, z128.x, z128.y};
  return k;
}

DI void kgen_steps(const char* pbase, int lane, f32x4 (&acc)[2][2]) {
  const int row = lane & 15, g = lane >> 4;
  const float tauA = (float)(row < 4 ? row : (row == 4 ? 256 : (row == 5 ? 258 : (row <= 12 ? (row - 4) * 256 : 0))));
  const float ntauA = -tauA, tauB = (float)(8 * row);
#pragma unroll 1
  for (int ks = 0; ks < 4; ++ks) {
    const char* pk = pbase + ks * 512 + g * 128;
    f32x4 P0[4], P1[4];
#pragma unroll
    for (int i = 0; i < 4; ++i) { P0[i] = *(const f32x4*)(pk + i * 32); P1[i] = *(const f32x4*)(pk + i * 32 + 16); }
    u32x4 A[2], B[2];
    float ma[4], mb[4], fa[4], fb[4], Tr[4][4], Ti[4][4], t1[4], t2[4];
#pragma unroll
    for (int i = 0; i < 4; ++i) { ma[i] = smul(P0[i].x, tauA); fa[i] = smul(P0[i].y, ntauA); mb[i] = smul(P0[i].x, tauB); fb[i] = smul(P0[i].y, tauB); }
    SB();
#pragma unroll
    for (int i = 0; i < 4; ++i) { fa[i] = __builtin_amdgcn_fractf(fa[i]); fb[i] = __builtin_amdgcn_fractf(fb[i]); }
#pragma unroll
    for (int i = 0; i < 4; ++i) { ma[i] = __builtin_amdgcn_exp2f(ma[i]); mb[i] = __builtin_amdgcn_exp2f(mb[i]); }
    SB();
#pragma unroll
    for (int i = 0; i < 4; ++i) {
      t1[i] = __builtin_amdgcn_cosf(fa[i]); t2[i] = __builtin_amdgcn_sinf(fa[i]);
      Tr[2][i] = __builtin_amdgcn_cosf(fb[i]); Ti[2][i] = __builtin_amdgcn_sinf(fb[i]);
    }
    SB();
#pragma unroll
    for (int i = 0; i < 4; ++i) { t1[i] = smul(t1[i], ma[i]); t2[i] = smul(t2[i], ma[i]); Tr[2][i] = smul(Tr[2][i], mb[i]); Ti[2][i] = smul(Ti[2][i], mb[i]); }
    SB();
#pragma unroll
    for (int i = 0; i < 4; ++i) { Tr[0][i] = smul(P0[i].z, t1[i]); Ti[0][i] = smul(P0[i].z, t2[i]); Tr[3][i] = smul(Tr[2][i], P1[i].z); Ti[3][i] = smul(Tr[2][i], P1[i].w); }
    SB();
#pragma unroll
    for (int i = 0; i < 4; ++i) {
      Tr[0][i] = sfnma(P0[i].w, t2[i], Tr[0][i]); Ti[0][i] = sfma(P0[i].w, t1[i], Ti[0][i]);
      Tr[3][i] = sfnma(Ti[2][i], P1[i].w, Tr[3][i]); Ti[3][i] = sfma(Ti[2][i], P1[i].z, Ti[3][i]);
    }
    SB();
#pragma unroll
    for (int i = 0; i < 4; ++i) { Tr[1][i] = smul(Tr[0][i], P1[i].x); Ti[1][i] = smul(Tr[0][i], P1[i].y); B[0][i] = cvtpkh(Tr[2][i], Ti[2][i]); B[1][i] = cvtpkh(Tr[3][i], Ti[3][i]); }
    SB();
#pragma unroll
    for (int i = 0; i < 4; ++i) { Tr[1][i] = sfnma(Ti[0][i], P1[i].y, Tr[1][i]); Ti[1][i] = sfma(Ti[0][i], P1[i].x, Ti[1][i]); A[0][i] = cvtpkh(Tr[0][i], Ti[0][i]); }
    SB();
    acc[0][0] = MFMA16H(A[0], B[0], acc[0][0]);
    acc[0][1] = MFMA16H(A[0], B[1], acc[0][1]);
#pragma unroll
    for (int i = 0; i < 4; ++i) A[1][i] = cvtpkh(Tr[1][i], Ti[1][i]);
    SB();
    acc[1][0] = MFMA16H(A[1], B[0], acc[1][0]);
    acc[1][1] = MFMA16H(A[1], B[1], acc[1][1]);
  }
}

constexpr int A_STRIDE = 912;
constexpr int G1_A_BYTES = 128 * A_STRIDE;
constexpr int G1_B_OFF = G1_A_BYTES;
constexpr int G1_K_OFF = G1_B_OFF + 32768;
constexpr int G1_LDS = G1_K_OFF + 14336;

__global__ __launch_bounds__(512, 2) void k_l1(const u16* __restrict__ x, const u16* __restrict__ wg, const u16* __restrict__ wu,
                                               const u16* __restrict__ wd, const u16* __restrict__ Are, const u16* __restrict__ Aim,
                                               const u16* __restrict__ Bre, const u16* __restrict__ Bim, const u16* __restrict__ Cre,
                                               const u16* __restrict__ Cim, const u16* __restrict__ ldt, char* __restrict__ ws) {
  extern __shared__ __attribute__((aligned(16))) char sm[];
  const int tid = TIDX, lane = tid & 63;
  const int wave = __builtin_amdgcn_readfirstlane(tid >> 6);
  const int b = BIDX;
  u16* Kg = (u16*)(ws + WS_K);
  const int wq = wave & 3;

  const int xcd = b & 7, idx = b >> 3;
  const int mt = (xcd & 3) * 4 + (idx & 3), nt = (xcd >> 2) * 7 + (idx >> 2);
  u16* hfrag = (u16*)(ws + WS_H);
  u16* wdf = (u16*)(ws + WS_WD);
  u16* xnT = (u16*)(ws + WS_XT);
  RSTAMP(0, 15); STAMP(0, 0); RSTAMP(3, 15); STAMP(3, 0);

  if (b == 0 && tid < 7) *(unsigned*)(ws + WS_CNT + tid * 256) = 0u;
  if (tid < 4) *(unsigned*)(sm + 896 + tid * 4) = 0u;

  const int sub = lane & 7;
  u32x4 raw[2][7];
#pragma unroll
  for (int p = 0; p < 2; ++p) {
    const int row = 16 * wave + 8 * p + (lane >> 3);
    const u32x4* xp = (const u32x4*)(x + (size_t)(mt * 128 + row) * D_);
#pragma unroll
    for (int j = 0; j < 7; ++j) raw[p][j] = xp[sub + 8 * j];
  }

  const int dch = 2 * b + (wq >> 1), hf = wq & 1;
  char* kreg = sm + G1_K_OFF + (wq >> 1) * 7168;
  unsigned rt[2][8];
  u32x4 tapo[2];
  u16 pa[2][2], pb[4][4], pl = 0;
  if (wave >= 4) {
#pragma unroll
    for (int i = 0; i < 4; ++i) {
      const int n = dch * N_ + ((hf * 128 + i * 64 + lane) & 255);
      pb[i][0] = Bre[n]; pb[i][1] = Bim[n]; pb[i][2] = Cre[n]; pb[i][3] = Cim[n];
      if (i < 2) { pa[i][0] = Are[n]; pa[i][1] = Aim[n]; }
    }
    pl = ldt[dch];
#pragma unroll
    for (int i = 0; i < 2; ++i) {
      int fr = b * 7 + wq + 4 * i;
      if (wq + 4 * i >= 7) fr = b * 7;
      const int fnt = fr / 112, fks = fr % 112, h = lane >> 5;
#pragma unroll
      for (int j = 0; j < 8; ++j) rt[i][j] = wd[(size_t)(16 * fks + 8 * (j >> 2) + 4 * h + (j & 3)) * D_ + 32 * fnt + (lane & 31)];
    }
    const u32x4* tp = (const u32x4*)(g_taptab.v + hf * 1024 + lane * 16);
    tapo[0] = tp[0]; tapo[1] = tp[1];
  }

  const u16* srcg = wg + (size_t)(lane >> 2) * F_ + nt * 128 + wq * 32 + (lane & 3) * 8;
  const u16* srcu = wu + (size_t)(lane >> 2) * F_ + nt * 128 + wq * 32 + (lane & 3) * 8;
  const unsigned bdst = (unsigned)(size_t)(LAS char*)sm + G1_B_OFF + wq * 8192;
  auto glds = [&](const u16* src, unsigned dst) {
    unsigned keep;
    asm volatile("s_mov_b32 %0, m0\n\ts_mov_b32 m0, %2\n\ts_nop 0\n\tglobal_load_lds_dwordx4 %1, off\n\ts_mov_b32 m0, %0"
                 : "=&s"(keep) : "v"(src), "s"(dst) : "memory");
  };

#pragma unroll
  for (int p = 0; p < 2; ++p) {
    const int row = 16 * wave + 8 * p + (lane >> 3);
    f32x2 xf[28];
    f32x2 s2 = {0.f, 0.f};
#pragma unroll
    for (int j = 0; j < 7; ++j)
#pragma unroll
      for (int e = 0; e < 4; ++e) { unsigned u = raw[p][j][e]; f32x2 v = {lo16(u), hi16(u)}; xf[j * 4 + e] = v; s2 += v; }
    float mean = red8(s2.x + s2.y) * (1.0f / 448.0f);
    float m = rbf(mean);
    f32x2 q2 = {0.f, 0.f};
    f32x2 mm = {m, m};
#pragma unroll
    for (int i = 0; i < 28; ++i) { f32x2 d = xf[i] - mm; xf[i] = d; q2 += d * d; }
    float dl = mean - m;
    float var = red8(q2.x + q2.y) * (1.0f / 448.0f) - dl * dl;
    var = __builtin_fmaxf(var, 0.f);
    float sd = rbf(__builtin_amdgcn_sqrtf(rbf(rbf(var) + 1e-5f)));
    float r = __builtin_amdgcn_rcpf(sd);
#pragma unroll
    for (int j = 0; j < 7; ++j) {
      u32x4 o;
#pragma unroll
      for (int e = 0; e < 4; ++e) o[e] = cvtpk(rbf(xf[j * 4 + e].x) * r, rbf(xf[j * 4 + e].y) * r);
      *(u32x4*)(sm + row * A_STRIDE + (sub + 8 * j) * 16) = o;
    }
    if (p == 0) {
      STAMP(0, 1);
      WAITV(0);
      if (wave >= 4) {
#pragma unroll
        for (int i = 0; i < 2; ++i) {
          if (wq + 4 * i < 7) {
            u32x4 o = {rt[i][0] | (rt[i][1] << 16), rt[i][2] | (rt[i][3] << 16), rt[i][4] | (rt[i][5] << 16), rt[i][6] | (rt[i][7] << 16)};
            *(u32x4*)((char*)wdf + ((size_t)(b * 7 + wq + 4 * i) * 64 + lane) * 16) = o;
          }
        }
      }
      if (wave < 4) {
#pragma unroll
        for (int s = 0; s < 4; ++s) { glds(srcg + (size_t)s * 16 * F_, bdst + s * 2048); glds(srcu + (size_t)s * 16 * F_, bdst + s * 2048 + 1024); }
      }
      SB();
    }
  }
  STAMP(0, 2);
  SYNC();
  STAMP(0, 3); STAMP(3, 3);

  if (wave >= 4) {
    {
      const int gq = lane >> 4, q = (lane & 15) >> 2, p = lane & 3;
      const unsigned base = (32 * wq + 16 * (gq >> 1) + q) * A_STRIDE + (32 * nt + 16 * (gq & 1) + 4 * p) * 2;
      s16x4 v[4];
#pragma unroll
      for (int i = 0; i < 4; ++i) v[i] = __builtin_amdgcn_ds_read_tr16_b64_v4i16((LAS s16x4*)(sm + base + 4 * i * A_STRIDE));
      u16* dst = xnT + (size_t)(32 * nt + 16 * (gq & 1) + (lane & 15)) * L_ + mt * 128 + 32 * wq + 16 * (gq >> 1);
#pragma unroll
      for (int c = 0; c < 2; ++c) {
        u32x4 o;
#pragma unroll
        for (int e = 0; e < 4; ++e) {
          const s16x4 w = v[2 * c + (e >> 1)];
          o[e] = cvtpkh(bf2f((u16)w[2 * (e & 1)]), bf2f((u16)w[2 * (e & 1) + 1]));
        }
        *(u32x4*)(dst + 8 * c) = o;
      }
    }
    STAMP(3, 4);
    const float dt = rbf(exp_acc(bf2f(pl)));
    float wr[4], wi[4], mx = 0.f;
#pragma unroll
    for (int i = 0; i < 4; ++i) {
      float br = dt * bf2f(pb[i][0]), bi = dt * bf2f(pb[i][1]), cr = bf2f(pb[i][2]), ci = bf2f(pb[i][3]);
      wr[i] = cr * br - ci * bi; wi[i] = cr * bi + ci * br;
      mx = __builtin_fmaxf(mx, __builtin_fmaxf(__builtin_fabsf(wr[i]), __builtin_fabsf(wi[i])));
    }
    mx = wave_max(mx);
    unsigned eb = (F2U(mx) >> 23) & 255u;
    eb = eb < 2u ? 126u : (eb > 250u ? 250u : eb);
    const float sc = U2F((253u - eb) << 23);
    const float kscale = U2F((eb + 1u) << 23);
    KP k0 = kparams(dt, sc, pa[0][0], pa[0][1], wr[0], wi[0]);
    KP kp1 = kparams(dt, sc, pa[1][0], pa[1][1], wr[1], wi[1]);
    *(f32x4*)(kreg + hf * 2048 + lane * 32) = k0.p0;
    *(f32x4*)(kreg + hf * 2048 + lane * 32 + 16) = k0.p1;
    __builtin_amdgcn_wave_barrier();
    f32x4 kacc[2][2];
#pragma unroll
    for (int a = 0; a < 2; ++a)
#pragma unroll
      for (int bb = 0; bb < 2; ++bb) kacc[a][bb] = f32x4{0.f, 0.f, 0.f, 0.f};
    char* pbase = kreg + hf * 2048;
    kgen_steps(pbase, lane, kacc);
    __builtin_amdgcn_wave_barrier();
    *(f32x4*)(pbase + lane * 32) = kp1.p0;
    *(f32x4*)(pbase + lane * 32 + 16) = kp1.p1;
    __builtin_amdgcn_wave_barrier();
    kgen_steps(pbase, lane, kacc);
    STAMP(3, 5);
    char* tab = kreg + 4096;
    volatile LAS unsigned* flag = (volatile LAS unsigned*)(LAS char*)(sm + 896 + (wq >> 1) * 8);
    if (hf == 1) { tab_store(tab, lane, kacc); flag_set(flag); flag_wait(flag + 1); }
    else { flag_wait(flag); tab_add(tab, lane, kacc); tab_store(tab, lane, kacc); flag_set(flag + 1); }
    STAMP(3, 6);
    u16* kh = Kg + (size_t)dch * KL;
#pragma unroll
    for (int c = 0; c < 2; ++c) {
      u32x4 o;
#pragma unroll
      for (int e = 0; e < 4; ++e) {
        const unsigned to = tapo[c][e];
        o[e] = cvtpkh(*(const float*)(tab + (to & 0xffffu)), *(const float*)(tab + (to >> 16)));
      }
      *(u32x4*)(kh + hf * 1024 + lane * 16 + c * 8) = o;
    }
    if (hf == 0 && lane < 8) { u32x4 z = {0, 0, 0, 0}; *(u32x4*)(kh + 2048 + 8 * lane) = z; }
    if (hf == 1 && lane == 0) *(float*)(ws + WS_KS + dch * 4) = kscale;
    STAMP(3, 7); RSTAMP(3, 14);
    return;
  }

  const int h = lane >> 5, blk = (lane >> 4) & 1, q = (lane & 15) >> 2, p = lane & 3;
  const unsigned tra = G1_B_OFF + wave * 8192 + 64 * (8 * h + q) + 32 * blk + 8 * p;
  f32x16 acc[4][2];
#pragma unroll
  for (int a = 0; a < 4; ++a)
#pragma unroll
    for (int g = 0; g < 2; ++g)
#pragma unroll
      for (int i = 0; i < 16; ++i) acc[a][g][i] = 0.f;
  bf16x8 xf[2][4], wf_[2][2];
  unsigned xa = (lane & 31) * A_STRIDE + 16 * h;
  auto rdx = [&](int u, int buf, int tt) { xf[buf][tt] = *(const bf16x8*)(sm + xa + tt * 32 * A_STRIDE + u * 32); };
  auto rdw = [&](int u, int buf, int g) {
    s16x4 a0 = __builtin_amdgcn_ds_read_tr16_b64_v4i16((LAS s16x4*)(sm + tra + (u & 3) * 2048 + g * 1024));
    s16x4 a1 = __builtin_amdgcn_ds_read_tr16_b64_v4i16((LAS s16x4*)(sm + tra + (u & 3) * 2048 + g * 1024 + 256));
    wf_[buf][g] = __builtin_shufflevector(a0, a1, 0, 1, 2, 3, 4, 5, 6, 7);
  };
#define MM(tt, g) acc[tt][g] = MFMA32(wf_[u & 1][g], xf[u & 1][tt], acc[tt][g])
  WAITV(6);
  rdw(0, 0, 0); rdw(0, 0, 1); rdx(0, 0, 0); rdx(0, 0, 1); rdx(0, 0, 2); rdx(0, 0, 3);
  SB();
#pragma unroll 1
  for (int it = 0; it < 7; ++it) {
    const int nx = it < 6 ? it + 1 : 6;
    const u16* sg = srcg + (size_t)nx * 64 * F_;
    const u16* su = srcu + (size_t)nx * 64 * F_;
#pragma unroll
    for (int u = 0; u < 4; ++u) {
      const int nb = (u + 1) & 1;
      MM(0, 0); SB();
      WAITV(4);
      rdw(u + 1, nb, 0); rdw(u + 1, nb, 1);
      SB();
      MM(0, 1); SB();
      glds(sg + (size_t)u * 16 * F_, bdst + u * 2048);
      SB();
      MM(1, 0); SB();
      rdx(u + 1, nb, 0); rdx(u + 1, nb, 1);
      SB();
      MM(1, 1); SB();
      glds(su + (size_t)u * 16 * F_, bdst + u * 2048 + 1024);
      SB();
      MM(2, 0); SB();
      rdx(u + 1, nb, 2); rdx(u + 1, nb, 3);
      SB();
      MM(2, 1); SB();
      MM(3, 0); SB();
      MM(3, 1); SB();
    }
    xa += 128;
  }
  WAITV(0);
  STAMP(0, 4);
#pragma unroll
  for (int tt = 0; tt < 4; ++tt)
#pragma unroll
    for (int s = 0; s < 2; ++s) {
      u32x4 o;
#pragma unroll
      for (int e = 0; e < 4; ++e)
        o[e] = cvtpk(rbf(acc[tt][0][8 * s + 2 * e]) * rbf(acc[tt][1][8 * s + 2 * e]), rbf(acc[tt][0][8 * s + 2 * e + 1]) * rbf(acc[tt][1][8 * s + 2 * e + 1]));
      const int mtile = 4 * mt + tt, kstep = 8 * nt + 2 * wave + s;
      *(u32x4*)((char*)hfrag + ((size_t)(mtile * 112 + kstep) * 64 + lane) * 16) = o;
    }
  STAMP(0, 5); RSTAMP(0, 14);
}

constexpr int CS = 4160;
constexpr int CV_K_OFF = 512;
constexpr int CV_X_OFF = 33792;
constexpr int L2_LDS = 49152;
#define POLL_SLEEP 8

__global__ __launch_bounds__(256, 3) void k_l2(const u16* __restrict__ x, const u16* __restrict__ Dskip, char* __restrict__ ws,
                                               u16* __restrict__ out) {
  extern __shared__ __attribute__((aligned(16))) char sm[];
  const int tid = TIDX, lane = tid & 63;
  const int wave = __builtin_amdgcn_readfirstlane(tid >> 6);
  u16* aT = (u16*)(ws + WS_AT);
  if (BIDX < 448) {
    const int d = BIDX;
    const u16* kh = (const u16*)(ws + WS_K) + (size_t)d * KL + tid * 8;
    const u16* xnT = (const u16*)(ws + WS_XT);
    RSTAMP(2, 15); STAMP(2, 0);
    {
      u32x4 ka = *(const u32x4*)(kh);
      u32x2 kb = *(const u32x2*)(kh + 8);
      u32x4 xv = *(const u32x4*)(xnT + (size_t)d * L_ + tid * 8);
      u32x4 z = {0, 0, 0, 0};
      if (tid < 160) *(u32x4*)(sm + CV_X_OFF + tid * 16) = z;
      if (tid < 16) *(u32x4*)(sm + CV_K_OFF + (tid >> 2) * CS + 4096 + (tid & 3) * 16) = z;
      const unsigned a10 = __builtin_amdgcn_alignbit(ka[1], ka[0], 16), a21 = __builtin_amdgcn_alignbit(ka[2], ka[1], 16);
      const unsigned a32 = __builtin_amdgcn_alignbit(ka[3], ka[2], 16), a43 = __builtin_amdgcn_alignbit(kb[0], ka[3], 16);
      const unsigned a54 = __builtin_amdgcn_alignbit(kb[1], kb[0], 16);
      char* dst = sm + CV_K_OFF + tid * 16;
      *(u32x4*)(dst) = ka;
      *(u32x4*)(dst + CS) = u32x4{a10, a21, a32, a43};
      *(u32x4*)(dst + 2 * CS) = u32x4{ka[1], ka[2], ka[3], kb[0]};
      *(u32x4*)(dst + 3 * CS) = u32x4{a21, a32, a43, a54};
      *(u32x4*)(sm + CV_X_OFF + (32 + (tid >> 2)) * 80 + (tid & 3) * 16) = xv;
    }
    const float ksc = *(const float*)(ws + WS_KS + d * 4);
    const float ds = bf2f(Dskip[d]);
    SYNC();
    STAMP(2, 1);
    const int r = lane & 31, h = lane >> 5;
    const int cls = (3 - r) & 3;
    unsigned ao = CV_K_OFF + cls * CS + 2 * (2047 - 32 * wave - r + 8 * h - cls) - 256;
    unsigned bo = CV_X_OFF + (32 + r - wave) * 80 + 16 * h - 320;
    f32x16 acc0, acc1;
#pragma unroll
    for (int i = 0; i < 16; ++i) { acc0[i] = 0.f; acc1[i] = 0.f; }
    bf16x8 Af[2], B1[2], B0[2];
    auto rd = [&](int nxt, int kk, int buf, bool both) {
      const unsigned a_ = ao + (nxt ? 0 : 256) + 32 * kk;
      s16x4 h0 = *(const volatile LAS s16x4*)(LAS char*)(sm + a_), h1 = *(const volatile LAS s16x4*)(LAS char*)(sm + a_ + 8);
      Af[buf] = __builtin_shufflevector(h0, h1, 0, 1, 2, 3, 4, 5, 6, 7);
      const unsigned b_ = bo + (nxt ? 0 : 320) + 32 * kk;
      B1[buf] = *(const bf16x8*)(sm + b_ + 2560);
      if (both) B0[buf] = *(const bf16x8*)(sm + b_);
    };
    rd(0, 0, 0, true);
    SB();
#pragma unroll 1
    for (int q = 0; q < 8; ++q) {
      rd(0, 1, 1, true); SB();
      acc1 = MFMA32H(Af[0], B1[0], acc1);
      acc0 = MFMA32H(Af[0], B0[0], acc0); SB();
      rd(1, 0, 0, true); SB();
      acc1 = MFMA32H(Af[1], B1[1], acc1);
      acc0 = MFMA32H(Af[1], B0[1], acc0); SB();
      ao -= 256; bo -= 320;
    }
#pragma unroll 1
    for (int q = 8; q < 16; ++q) {
      rd(0, 1, 1, false); SB();
      acc1 = MFMA32H(Af[0], B1[0], acc1); SB();
      rd(1, 0, 0, false); SB();
      acc1 = MFMA32H(Af[1], B1[1], acc1); SB();
      ao -= 256; bo -= 320;
    }
    STAMP(2, 2);
    SYNC();
#pragma unroll
    for (int g = 0; g < 4; ++g) {
      *(f32x4*)(sm + wave * 8192 + g * 1024 + lane * 16) = f32x4{acc0[4 * g], acc0[4 * g + 1], acc0[4 * g + 2], acc0[4 * g + 3]};
      *(f32x4*)(sm + wave * 8192 + 4096 + g * 1024 + lane * 16) = f32x4{acc1[4 * g], acc1[4 * g + 1], acc1[4 * g + 2], acc1[4 * g + 3]};
    }
    SYNC();
    {
      const int g = tid >> 6, i = lane, tile = i >> 5, col = i & 31;
      f32x4 s0 = {0.f, 0.f, 0.f, 0.f}, s1 = {0.f, 0.f, 0.f, 0.f};
#pragma unroll
      for (int w = 0; w < 4; ++w) {
        s0 += *(const f32x4*)(sm + w * 8192 + tile * 4096 + g * 1024 + col * 16);
        s1 += *(const f32x4*)(sm + w * 8192 + tile * 4096 + g * 1024 + (col + 32) * 16);
      }
      u32x4 xv = *(const u32x4*)(sm + CV_X_OFF + (32 + i) * 80 + g * 16);
      float y[8] = {s0.x, s0.y, s0.z, s0.w, s1.x, s1.y, s1.z, s1.w};
      u32x4 o;
#pragma unroll
      for (int e = 0; e < 4; ++e) {
        const unsigned xe = xv[e];
        f16x2v xh = __builtin_bit_cast(f16x2v, xe);
        o[e] = cvtpk(rbf(y[2 * e] * ksc) + rbf((float)xh.x * ds), rbf(y[2 * e + 1] * ksc) + rbf((float)xh.y * ds));
      }
      *(u32x4*)(sm + 41984 + (i * 4 + g) * 16) = o;
    }
    SYNC();
    {
      u32x4 o = *(const u32x4*)(sm + 41984 + tid * 16);
      u16* dst = aT + (size_t)d * L_ + tid * 8;
      asm volatile("global_store_dwordx4 %0, %1, off sc1\n\ts_nop 1" ::"v"(dst), "v"(o) : "memory");
    }
    WAITV(0);
    SYNC();
    if (tid == 0) __hip_atomic_fetch_add((unsigned*)(ws + WS_CNT + (d >> 6) * 256), 1u, __ATOMIC_RELAXED, __HIP_MEMORY_SCOPE_AGENT);
    STAMP(2, 3); RSTAMP(2, 14);
    return;
  }
  const u16* hfrag = (const u16*)(ws + WS_H);
  const u16* wdf = (const u16*)(ws + WS_WD);
  const int b = BIDX - 448;
  const int xcd = b & 7, idx = b >> 3;
  const int mt2 = xcd * 4 + (idx & 3), nt2 = idx >> 2;
  const u32x4* hp = (const u32x4*)((const char*)hfrag + ((size_t)(2 * mt2) * 112 + 28 * wave) * 1024) + lane;
  const u32x4* wp = (const u32x4*)((const char*)wdf + ((size_t)(2 * nt2) * 112 + 28 * wave) * 1024) + lane;
  f32x16 acc[2][2];
#pragma unroll
  for (int i = 0; i < 2; ++i)
#pragma unroll
    for (int j = 0; j < 2; ++j)
#pragma unroll
      for (int e = 0; e < 16; ++e) acc[i][j][e] = 0.f;
  RSTAMP(4, 15); STAMP(4, 0);
  const int t = 64 * mt2 + 32 * (wave & 1) + (lane & 31), h = lane >> 5;
  const int d0 = 64 * nt2 + 32 * (wave >> 1) + 4 * h;
#define G2PRIO 0
  __builtin_amdgcn_s_setprio(G2PRIO);
#define G2DELAY 0
  if (G2DELAY) __builtin_amdgcn_s_sleep(G2DELAY);
  constexpr int DP = 4;
  u32x4 hb[DP][2], wb[DP][2];
#pragma unroll
  for (int ks = 0; ks < DP; ++ks) {
    hb[ks][0] = hp[ks * 64]; wb[ks][0] = wp[ks * 64]; wb[ks][1] = wp[(112 + ks) * 64]; hb[ks][1] = hp[(112 + ks) * 64];
  }
  u32x2 xv[4];
#pragma unroll
  for (int g = 0; g < 4; ++g) xv[g] = *(const u32x2*)(x + (size_t)t * D_ + d0 + 8 * g);
  SB();
#pragma unroll 1
  for (int it = 0; it < 6; ++it) {
    hp += DP * 64; wp += DP * 64;
#pragma unroll
    for (int sl = 0; sl < DP; ++sl) {
      acc[0][0] = MFMA32(BC8(wb[sl][0]), BC8(hb[sl][0]), acc[0][0]);
      acc[0][1] = MFMA32(BC8(wb[sl][1]), BC8(hb[sl][0]), acc[0][1]);
      acc[1][0] = MFMA32(BC8(wb[sl][0]), BC8(hb[sl][1]), acc[1][0]);
      acc[1][1] = MFMA32(BC8(wb[sl][1]), BC8(hb[sl][1]), acc[1][1]);
      SB();
      hb[sl][0] = hp[sl * 64]; wb[sl][0] = wp[sl * 64]; wb[sl][1] = wp[(112 + sl) * 64]; hb[sl][1] = hp[(112 + sl) * 64];
      SB();
    }
  }
#pragma unroll
  for (int sl = 0; sl < DP; ++sl) {
    acc[0][0] = MFMA32(BC8(wb[sl][0]), BC8(hb[sl][0]), acc[0][0]);
    acc[0][1] = MFMA32(BC8(wb[sl][1]), BC8(hb[sl][0]), acc[0][1]);
    acc[1][0] = MFMA32(BC8(wb[sl][0]), BC8(hb[sl][1]), acc[1][0]);
    acc[1][1] = MFMA32(BC8(wb[sl][1]), BC8(hb[sl][1]), acc[1][1]);
    SB();
  }
  STAMP(4, 1);
#pragma unroll
  for (int tl = 0; tl < 4; ++tl) {
    if (tl != wave) {
      const int slot = wave < tl ? wave : wave - 1;
#pragma unroll
      for (int g = 0; g < 4; ++g)
        *(f32x4*)(sm + (tl * 3 + slot) * 4096 + g * 1024 + lane * 16) =
            f32x4{acc[tl & 1][tl >> 1][4 * g], acc[tl & 1][tl >> 1][4 * g + 1], acc[tl & 1][tl >> 1][4 * g + 2], acc[tl & 1][tl >> 1][4 * g + 3]};
    }
  }
  if (wave == 0) {
    const unsigned* cnt = (const unsigned*)(ws + WS_CNT + nt2 * 256);
    for (unsigned spins = 0; spins < (1u << 20); ++spins) {
      unsigned c = __builtin_amdgcn_readfirstlane(__hip_atomic_load(cnt, __ATOMIC_RELAXED, __HIP_MEMORY_SCOPE_AGENT));
      if (c >= 64u) break;
      __builtin_amdgcn_s_sleep(POLL_SLEEP);
    }
  }
  SYNC();
  STAMP(4, 2);
  u16 av[16];
#pragma unroll
  for (int e = 0; e < 16; ++e)
    av[e] = __hip_atomic_load(aT + (size_t)(d0 + 8 * (e >> 2) + (e & 3)) * L_ + t, __ATOMIC_RELAXED, __HIP_MEMORY_SCOPE_AGENT);
  f32x16 own;
  if (wave == 0) own = acc[0][0]; else if (wave == 1) own = acc[1][0]; else if (wave == 2) own = acc[0][1]; else own = acc[1][1];
  float sum[16];
#pragma unroll
  for (int e = 0; e < 16; ++e) sum[e] = 0.f;
#pragma unroll
  for (int sw = 0; sw < 4; ++sw) {
    if (sw == wave) {
#pragma unroll
      for (int e = 0; e < 16; ++e) sum[e] += own[e];
    } else {
      const int slot = sw < wave ? sw : sw - 1;
#pragma unroll
      for (int g = 0; g < 4; ++g) {
        f32x4 v = *(const f32x4*)(sm + (wave * 3 + slot) * 4096 + g * 1024 + lane * 16);
        sum[4 * g] += v.x; sum[4 * g + 1] += v.y; sum[4 * g + 2] += v.z; sum[4 * g + 3] += v.w;
      }
    }
  }
#pragma unroll
  for (int g = 0; g < 4; ++g) {
    float o0 = rbf(lo16(xv[g].x) + bf2f(av[4 * g])) + rbf(sum[4 * g]);
    float o1 = rbf(hi16(xv[g].x) + bf2f(av[4 * g + 1])) + rbf(sum[4 * g + 1]);
    float o2 = rbf(lo16(xv[g].y) + bf2f(av[4 * g + 2])) + rbf(sum[4 * g + 2]);
    float o3 = rbf(hi16(xv[g].y) + bf2f(av[4 * g + 3])) + rbf(sum[4 * g + 3]);
    u32x2 o = {cvtpk(o0, o1), cvtpk(o2, o3)};
    *(u32x2*)(out + (size_t)t * D_ + d0 + 8 * g) = o;
  }
  STAMP(4, 3); RSTAMP(4, 14);
}

#if defined(__HIP_DEVICE_COMPILE__)
#pragma clang attribute pop
#endif

extern "C" void kernel_launch(void* const* d_in, const int* in_sizes, int n_in, void* d_out, int out_size, void* d_ws, size_t ws_size,
                              hipStream_t stream) {
  const u16* x = (const u16*)d_in[0];
  char* ws = (char*)d_ws;
  (void)hipFuncSetAttribute((const void*)k_l1, hipFuncAttributeMaxDynamicSharedMemorySize, G1_LDS);
  k_l1<<<224, 512, G1_LDS, stream>>>(x, (const u16*)d_in[10], (const u16*)d_in[11], (const u16*)d_in[12], (const u16*)d_in[1],
                                     (const u16*)d_in[2], (const u16*)d_in[3], (const u16*)d_in[4], (const u16*)d_in[5],
                                     (const u16*)d_in[6], (const u16*)d_in[7], ws);
  k_l2<<<672, 256, L2_LDS, stream>>>(x, (const u16*)d_in[8], ws, (u16*)d_out);
}
```

```cpp
#include <hip/hip_runtime.h>
#include <stdint.h>

#if defined(__HIP_DEVICE_COMPILE__)
#pragma clang attribute push(__attribute__((target("no-packed-fp32-ops"))), apply_to = function)
#endif
#define U2F(x) __builtin_bit_cast(float, (unsigned)(x))
#define F2U(x) __builtin_bit_cast(unsigned, (float)(x))
#define I2F(x) __builtin_bit_cast(float, (int)(x))
#define F2I(x) __builtin_bit_cast(int, (float)(x))
#define TIDX ((int)__builtin_amdgcn_workitem_id_x())
#define BIDX ((int)__builtin_amdgcn_workgroup_id_x())
#define SYNC() do { __builtin_amdgcn_fence(__ATOMIC_RELEASE, "workgroup"); __builtin_amdgcn_s_barrier(); __builtin_amdgcn_fence(__ATOMIC_ACQUIRE, "workgroup"); } while (0)

typedef unsigned short u16;
typedef short bf16x8 __attribute__((ext_vector_type(8)));
typedef short s16x4 __attribute__((ext_vector_type(4)));
typedef float f32x16 __attribute__((ext_vector_type(16)));
typedef float f32x4 __attribute__((ext_vector_type(4)));
typedef float f32x2 __attribute__((ext_vector_type(2)));
typedef unsigned u32x4 __attribute__((ext_vector_type(4)));
typedef unsigned u32x2 __attribute__((ext_vector_type(2)));
typedef __bf16 bf16x2v __attribute__((ext_vector_type(2)));
typedef _Float16 f16x2v __attribute__((ext_vector_type(2)));
typedef _Float16 f16x8 __attribute__((ext_vector_type(8)));
#define LAS __attribute__((address_space(3)))
#define DI __device__ __forceinline__

constexpr int L_ = 2048, D_ = 448, N_ = 256, F_ = 1792;
constexpr int KL = 2112;

DI float bf2f(u16 v) { return U2F(((unsigned)v) << 16); }
DI unsigned cvtpk(float a, float b) { f32x2 p = {a, b}; return __builtin_bit_cast(unsigned, __builtin_convertvector(p, bf16x2v)); }
DI unsigned cvtpkh(float a, float b) { f32x2 p = {a, b}; return __builtin_bit_cast(unsigned, __builtin_convertvector(p, f16x2v)); }
DI float exp_acc(float l) {
  const float t = l * 1.4426950408889634f;
  const float r = __builtin_fmaf(l, 1.4426950408889634f, -t) + l * 1.9259629911e-8f;
  const float e = __builtin_amdgcn_exp2f(t);
  return __builtin_fmaf(e, r * 0.6931471805599453f, e);
}
DI float rbf(float f) { return U2F(cvtpk(0.f, f)); }
DI float lo16(unsigned u) { return U2F(u << 16); }
DI float hi16(unsigned u) { return U2F(u & 0xffff0000u); }
#define DPPF(v, ctrl) __builtin_bit_cast(float, __builtin_amdgcn_mov_dpp(__builtin_bit_cast(int, (v)), (ctrl), 0xF, 0xF, true))
DI float red8(float v) { v += DPPF(v, 0xB1); v += DPPF(v, 0x4E); v += DPPF(v, 0x141); return v; }
#define MFMA32(a, b, c) __builtin_amdgcn_mfma_f32_32x32x16_bf16((a), (b), (c), 0, 0, 0)
#define MFMA16H(a, b, c) __builtin_amdgcn_mfma_f32_16x16x32_f16(__builtin_bit_cast(f16x8, (a)), __builtin_bit_cast(f16x8, (b)), (c), 0, 0, 0)
#define MFMA32H(a, b, c) __builtin_amdgcn_mfma_f32_32x32x16_f16(__builtin_bit_cast(f16x8, (a)), __builtin_bit_cast(f16x8, (b)), (c), 0, 0, 0)
#define WAITV(n) asm volatile("s_waitcnt vmcnt(" #n ")" ::: "memory")
#define WAITL(n) asm volatile("s_waitcnt lgkmcnt(" #n ")" ::: "memory")
#define SB() __builtin_amdgcn_sched_barrier(0)
#define BC8(x) __builtin_bit_cast(bf16x8, (x))

DI void st_wt(void* p, u32x4 v) { asm volatile("global_store_dwordx4 %0, %1, off sc1\n\ts_nop 1" ::"v"(p), "v"(v) : "memory"); }

constexpr size_t WS_CNT = 0;
constexpr size_t WS_H = 4096;
constexpr size_t WS_WD = WS_H + (size_t)64 * 112 * 1024;
constexpr size_t WS_XT = WS_WD + (size_t)14 * 112 * 1024;
constexpr size_t WS_K = WS_XT + (size_t)D_ * L_ * 2;
constexpr size_t WS_AT = WS_K + (size_t)D_ * KL * 2;
constexpr size_t WS_KS = WS_AT + (size_t)D_ * L_ * 2;
constexpr size_t WS_K2 = WS_KS + 4096;

struct TapTab { u16 v[2048]; };
constexpr TapTab make_taptab() {
  TapTab t{};
  for (int i = 0; i < 2048; ++i) {
    int l = 2047 - i;
    int sh = l < 256 ? 0 : (l < 512 ? 1 : (l < 1024 ? 2 : 3));
    int v = l;
    if (sh) {
      int q = l >> sh, r = l & ((1 << sh) - 1), hf = 1 << (sh - 1);
      if (r > hf || (r == hf && (q & 1))) q += 1;
      v = q << sh;
    }
    int cp = (v & 255) >> 3, a = (v >> 2) & 1, h8 = v >> 8;
    int row = h8 == 0 ? (v & 3) : (h8 == 1 ? 4 + ((v >> 1) & 1) : 4 + h8);
    int bb = cp >> 4, col = cp & 15, ln = col + 16 * (row >> 2);
    int off = (a == 0 ? bb * 1024 : 2048 + bb * 512) + ln * 16 + (row & 3) * 4;
    t.v[i] = (u16)off;
  }
  return t;
}
__device__ const TapTab g_taptab = make_taptab();

DI float smul(float a, float b) { float r; asm("v_mul_f32 %0, %1, %2" : "=v"(r) : "v"(a), "v"(b)); return r; }
DI float ssub(float a, float b) { float r; asm("v_sub_f32 %0, %1, %2" : "=v"(r) : "v"(a), "v"(b)); return r; }
DI float sfma(float a, float b, float c) { float r; asm("v_fma_f32 %0, %1, %2, %3" : "=v"(r) : "v"(a), "v"(b), "v"(c)); return r; }
DI float sfnma(float a, float b, float c) { float r; asm("v_fma_f32 %0, -%1, %2, %3" : "=v"(r) : "v"(a), "v"(b), "v"(c)); return r; }
DI f32x2 cexp2(float e, float ph) {
  float m = __builtin_amdgcn_exp2f(e);
  float fr = __builtin_amdgcn_fractf(ph);
  return f32x2{m * __builtin_amdgcn_cosf(fr), m * __builtin_amdgcn_sinf(fr)};
}

DI void flag_wait(volatile LAS unsigned* f) {
  while (*f == 0u) __builtin_amdgcn_s_sleep(1);
  asm volatile("" ::: "memory");
}
DI void flag_set(volatile LAS unsigned* f) {
  WAITL(0);
  *f = 1u;
}
DI void tab_store(char* t, int lane, const f32x4 (&acc)[2][2]) {
  *(f32x4*)(t + lane * 16) = acc[0][0];
  *(f32x4*)(t + 1024 + lane * 16) = acc[0][1];
  if (lane < 32) { *(f32x4*)(t + 2048 + lane * 16) = acc[1][0]; *(f32x4*)(t + 2560 + lane * 16) = acc[1][1]; }
}
DI void tab_add(const char* t, int lane, f32x4 (&acc)[2][2]) {
  acc[0][0] += *(const f32x4*)(t + lane * 16);
  acc[0][1] += *(const f32x4*)(t + 1024 + lane * 16);
  acc[1][0] += *(const f32x4*)(t + 2048 + (lane & 31) * 16);
  acc[1][1] += *(const f32x4*)(t + 2560 + (lane & 31) * 16);
}
DI float wave_max(float v) {
#define DPPM(ctrl) v = __builtin_fmaxf(v, __builtin_bit_cast(float, __builtin_amdgcn_update_dpp(__builtin_bit_cast(int, v), __builtin_bit_cast(int, v), (ctrl), 0xF, 0xF, false)))
  DPPM(0xB1); DPPM(0x4E); DPPM(0x141); DPPM(0x140);
#undef DPPM
  float a = __builtin_bit_cast(float, __builtin_amdgcn_readlane(__builtin_bit_cast(int, v), 0));
  float b = __builtin_bit_cast(float, __builtin_amdgcn_readlane(__builtin_bit_cast(int, v), 16));
  float c = __builtin_bit_cast(float, __builtin_amdgcn_readlane(__builtin_bit_cast(int, v), 32));
  float d = __builtin_bit_cast(float, __builtin_amdgcn_readlane(__builtin_bit_cast(int, v), 48));
  return __builtin_fmaxf(__builtin_fmaxf(a, b), __builtin_fmaxf(c, d));
}
struct KP { f32x4 p0; u32x2 p1; };
DI float atan2_rev(float wi, float wr) {
  const float ax = __builtin_fabsf(wr), ay = __builtin_fabsf(wi);
  const float mx = __builtin_fmaxf(ax, ay), mn = __builtin_fminf(ax, ay);
  const float t = mx > 0.f ? mn * __builtin_amdgcn_rcpf(mx) : 0.f, s = t * t;
  float p = 1.0841296645e-03f;
  p = __builtin_fmaf(p, s, -5.3482756860e-03f);
  p = __builtin_fmaf(p, s, 1.2672499728e-02f);
  p = __builtin_fmaf(p, s, -2.1061518382e-02f);
  p = __builtin_fmaf(p, s, 3.1525118139e-02f);
  p = __builtin_fmaf(p, s, -5.3026238329e-02f);
  p = __builtin_fmaf(p, s, 1.5915432424e-01f);
  p *= t;
  p = ay > ax ? 0.25f - p : p;
  p = wr < 0.f ? 0.5f - p : p;
  return wi < 0.f ? -p : p;
}
DI KP kparams(float dt, float sc, u16 are, u16 aim, float wr, float wi) {
  float ar2 = dt * bf2f(are) * 1.4426950408889634f, ai2 = dt * bf2f(aim) * 0.15915494309189535f;
  f32x2 z4 = cexp2(ar2 * 4.f, ai2 * 4.f), z128 = cexp2(ar2 * 128.f, ai2 * 128.f);
  const float a = wr * sc, c = wi * sc;
  KP k;
  k.p0 = f32x4{ar2, ai2, 0.5f * __builtin_amdgcn_logf(a * a + c * c), -atan2_rev(c, a)};
  k.p1 = u32x2{cvtpkh(z4.x, -z4.y), cvtpkh(z128.x, z128.y)};
  return k;
}
DI unsigned mulpack(float m, float c, float s) {
  unsigned r;
  asm("v_fma_mixlo_f16 %0, %1, %2, 0" : "=v"(r) : "v"(m), "v"(c));
  asm("v_fma_mixhi_f16 %0, %1, %2, 0" : "+v"(r) : "v"(m), "v"(s));
  return r;
}
DI unsigned cmulh1(unsigned a, unsigned b) { unsigned t; asm("v_pk_mul_f16 %0, %1, %2 op_sel:[0,0] op_sel_hi:[0,1]" : "=v"(t) : "v"(a), "v"(b)); return t; }
DI unsigned cmulh2(unsigned a, unsigned b, unsigned t) {
  unsigned r;
  asm("v_pk_fma_f16 %0, %1, %2, %3 op_sel:[1,1,0] op_sel_hi:[1,0,1] neg_lo:[1,0,0]" : "=v"(r) : "v"(a), "v"(b), "v"(t));
  return r;
}

template <int NS> DI void kgen_steps(const char* pbase, int lane, f32x4 (&acc)[2][2]) {
  const int row = lane & 15, g = lane >> 4;
  const float tauA = (float)(row < 4 ? row : (row == 4 ? 256 : (row == 5 ? 258 : (row <= 12 ? (row - 4) * 256 : 0))));
  const float ntauA = -tauA, tauB = (float)(8 * row);
#pragma unroll 1
  for (int ks = 0; ks < NS; ++ks) {
    const char* pk = pbase + ks * 512 + g * 128;
    f32x4 P0[4];
    u32x2 P1[4];
#pragma unroll
    for (int i = 0; i < 4; ++i) { P0[i] = *(const f32x4*)(pk + i * 32); P1[i] = *(const u32x2*)(pk + i * 32 + 16); }
    u32x4 A[2], B[2];
    float ma[4], mb[4], fa[4], fb[4], ca[4], sa[4], cb[4], sb[4];
    unsigned t[8];
#pragma unroll
    for (int i = 0; i < 4; ++i) { fa[i] = sfma(P0[i].y, ntauA, P0[i].w); fb[i] = smul(P0[i].y, tauB); ma[i] = sfma(P0[i].x, tauA, P0[i].z); mb[i] = smul(P0[i].x, tauB); }
    SB();
#pragma unroll
    for (int i = 0; i < 4; ++i) { fa[i] = __builtin_amdgcn_fractf(fa[i]); fb[i] = __builtin_amdgcn_fractf(fb[i]); }
#pragma unroll
    for (int i = 0; i < 4; ++i) { ma[i] = __builtin_amdgcn_exp2f(ma[i]); mb[i] = __builtin_amdgcn_exp2f(mb[i]); }
    SB();
#pragma unroll
    for (int i = 0; i < 4; ++i) {
      ca[i] = __builtin_amdgcn_cosf(fa[i]); sa[i] = __builtin_amdgcn_sinf(fa[i]);
      cb[i] = __builtin_amdgcn_cosf(fb[i]); sb[i] = __builtin_amdgcn_sinf(fb[i]);
    }
    SB();
#pragma unroll
    for (int i = 0; i < 4; ++i) { A[0][i] = mulpack(ma[i], ca[i], sa[i]); B[0][i] = mulpack(mb[i], cb[i], sb[i]); }
    SB();
#pragma unroll
    for (int i = 0; i < 4; ++i) { const unsigned a0 = A[0][i], b0 = B[0][i], z4 = P1[i].x, z8 = P1[i].y; t[i] = cmulh1(a0, z4); t[4 + i] = cmulh1(b0, z8); }
    SB();
#pragma unroll
    for (int i = 0; i < 4; ++i) { const unsigned a0 = A[0][i], b0 = B[0][i], z4 = P1[i].x, z8 = P1[i].y; A[1][i] = cmulh2(a0, z4, t[i]); B[1][i] = cmulh2(b0, z8, t[4 + i]); }
    SB();
    __builtin_amdgcn_s_setprio(2);
    asm volatile("s_nop 1");
    acc[0][0] = MFMA16H(A[0], B[0], acc[0][0]);
    acc[0][1] = MFMA16H(A[0], B[1], acc[0][1]);
    acc[1][0] = MFMA16H(A[1], B[0], acc[1][0]);
    acc[1][1] = MFMA16H(A[1], B[1], acc[1][1]);
    __builtin_amdgcn_s_setprio(0);
    SB();
  }
}

constexpr int A_STRIDE = 912;
constexpr int G1_A_BYTES = 128 * A_STRIDE;
constexpr int G1_B_OFF = G1_A_BYTES;
constexpr int G1_K_OFF = G1_B_OFF + 32768;
constexpr int G1_LDS = G1_K_OFF + 14336;

__global__ __launch_bounds__(512, 2) void k_l1(const u16* __restrict__ x, const u16* __restrict__ wg, const u16* __restrict__ wu,
                                               const u16* __restrict__ wd, const u16* __restrict__ Are, const u16* __restrict__ Aim,
                                               const u16* __restrict__ Bre, const u16* __restrict__ Bim, const u16* __restrict__ Cre,
                                               const u16* __restrict__ Cim, const u16* __restrict__ ldt, char* __restrict__ ws) {
  extern __shared__ __attribute__((aligned(16))) char sm[];
  const int tid = TIDX, lane = tid & 63;
  const int wave = __builtin_amdgcn_readfirstlane(tid >> 6);
  const int b = BIDX;
  u16* Kg = (u16*)(ws + WS_K);
  const int wq = wave & 3;

  if (b >= 224) {
    u16* K2 = (u16*)(ws + WS_K2);
    char* pbase = sm + wave * 8192;
    char* tab = pbase + 2048;
    u32x4 tp[4];
#pragma unroll
    for (int c = 0; c < 4; ++c) tp[c] = *(const u32x4*)(g_taptab.v + c * 512 + lane * 8);
    const int nr = wave < 6 ? 2 : 1;
#pragma unroll 1
    for (int r = 0; r < nr; ++r) {
      const int dch = 14 * (b - 224) + wave + 8 * r;
      u16 pb[4][4];
#pragma unroll
      for (int i = 0; i < 4; ++i) {
        const int n = dch * N_ + ((192 + i * 64 + lane) & 255);
        pb[i][0] = Bre[n]; pb[i][1] = Bim[n]; pb[i][2] = Cre[n]; pb[i][3] = Cim[n];
      }
      const u16 are = Are[dch * N_ + 192 + lane], aim = Aim[dch * N_ + 192 + lane];
      const float dt = rbf(exp_acc(bf2f(ldt[dch])));
      float wr[4], wi[4], mx = 0.f;
#pragma unroll
      for (int i = 0; i < 4; ++i) {
        float br = dt * bf2f(pb[i][0]), bi = dt * bf2f(pb[i][1]), cr = bf2f(pb[i][2]), ci = bf2f(pb[i][3]);
        wr[i] = cr * br - ci * bi; wi[i] = cr * bi + ci * br;
        mx = __builtin_fmaxf(mx, __builtin_fmaxf(__builtin_fabsf(wr[i]), __builtin_fabsf(wi[i])));
      }
      mx = wave_max(mx);
      unsigned eb = (F2U(mx) >> 23) & 255u;
      eb = eb < 2u ? 126u : (eb > 250u ? 250u : eb);
      const float sc = U2F((253u - eb) << 23);
      KP k0 = kparams(dt, sc, are, aim, wr[0], wi[0]);
      __builtin_amdgcn_wave_barrier();
      *(f32x4*)(pbase + lane * 32) = k0.p0;
      *(u32x2*)(pbase + lane * 32 + 16) = k0.p1;
      __builtin_amdgcn_wave_barrier();
      f32x4 kacc[2][2];
#pragma unroll
      for (int a = 0; a < 2; ++a)
#pragma unroll
        for (int bb = 0; bb < 2; ++bb) kacc[a][bb] = f32x4{0.f, 0.f, 0.f, 0.f};
      kgen_steps<4>(pbase, lane, kacc);
      tab_store(tab, lane, kacc);
      __builtin_amdgcn_wave_barrier();
      u16* kh = K2 + (size_t)dch * KL;
#pragma unroll
      for (int c = 0; c < 4; ++c) {
        u32x4 o;
#pragma unroll
        for (int e = 0; e < 4; ++e) {
          const unsigned to = tp[c][e];
          o[e] = cvtpkh(*(const float*)(tab + (to & 0xffffu)), *(const float*)(tab + (to >> 16)));
        }
        *(u32x4*)(kh + c * 512 + lane * 8) = o;
      }
      if (lane < 8) { u32x4 z = {0, 0, 0, 0}; *(u32x4*)(kh + 2048 + 8 * lane) = z; }
    }
    return;
  }

  const int xcd = b & 7, idx = b >> 3;
  const int mt = (xcd & 3) * 4 + (idx & 3), nt = (xcd >> 2) * 7 + (idx >> 2);
  u16* hfrag = (u16*)(ws + WS_H);
  u16* wdf = (u16*)(ws + WS_WD);
  u16* xnT = (u16*)(ws + WS_XT);

  if (b == 0 && tid < 7) *(unsigned*)(ws + WS_CNT + tid * 256) = 0u;
  if (tid < 4) *(unsigned*)(sm + 896 + tid * 4) = 0u;

  const int sub = lane & 7;
  u32x4 raw[2][7];
#pragma unroll
  for (int p = 0; p < 2; ++p) {
    const int row = 16 * wave + 8 * p + (lane >> 3);
    const u32x4* xp = (const u32x4*)(x + (size_t)(mt * 128 + row) * D_);
#pragma unroll
    for (int j = 0; j < 7; ++j) raw[p][j] = xp[sub + 8 * j];
  }

  const int dch = 2 * b + (wq >> 1), hf = wq & 1;
  char* kreg = sm + G1_K_OFF + (wq >> 1) * 7168;
  unsigned rt[2][8];
  u32x4 tapo[2];
  u16 pa[2][2], pb[4][4], pl = 0;
  if (wave >= 4) {
#pragma unroll
    for (int i = 0; i < 4; ++i) {
      const int n = dch * N_ + ((hf * 96 + i * 64 + lane) & 255);
      pb[i][0] = Bre[n]; pb[i][1] = Bim[n]; pb[i][2] = Cre[n]; pb[i][3] = Cim[n];
      if (i < 2) { pa[i][0] = Are[n]; pa[i][1] = Aim[n]; }
    }
    pl = ldt[dch];
#pragma unroll
    for (int i = 0; i < 2; ++i) {
      int fr = b * 7 + wq + 4 * i;
      if (wq + 4 * i >= 7) fr = b * 7;
      const int fnt = fr / 112, fks = fr % 112, h = lane >> 5;
#pragma unroll
      for (int j = 0; j < 8; ++j) rt[i][j] = wd[(size_t)(16 * fks + 8 * (j >> 2) + 4 * h + (j & 3)) * D_ + 32 * fnt + (lane & 31)];
    }
    const u32x4* tp = (const u32x4*)(g_taptab.v + hf * 1024 + lane * 16);
    tapo[0] = tp[0]; tapo[1] = tp[1];
  }

  const u16* srcg = wg + (size_t)(lane >> 2) * F_ + nt * 128 + wq * 32 + (lane & 3) * 8;
  const u16* srcu = wu + (size_t)(lane >> 2) * F_ + nt * 128 + wq * 32 + (lane & 3) * 8;
  const unsigned bdst = (unsigned)(size_t)(LAS char*)sm + G1_B_OFF + wq * 8192;
  auto glds = [&](const u16* src, unsigned dst) {
    unsigned keep;
    asm volatile("s_mov_b32 %0, m0\n\ts_mov_b32 m0, %2\n\ts_nop 0\n\tglobal_load_lds_dwordx4 %1, off\n\ts_mov_b32 m0, %0"
                 : "=&s"(keep) : "v"(src), "s"(dst) : "memory");
  };

#pragma unroll
  for (int p = 0; p < 2; ++p) {
    const int row = 16 * wave + 8 * p + (lane >> 3);
    f32x2 xf[28];
    f32x2 s2 = {0.f, 0.f};
#pragma unroll
    for (int j = 0; j < 7; ++j)
#pragma unroll
      for (int e = 0; e < 4; ++e) { unsigned u = raw[p][j][e]; f32x2 v = {lo16(u), hi16(u)}; xf[j * 4 + e] = v; s2 += v; }
    float mean = red8(s2.x + s2.y) * (1.0f / 448.0f);
    float m = rbf(mean);
    f32x2 q2 = {0.f, 0.f};
    f32x2 mm = {m, m};
#pragma unroll
    for (int i = 0; i < 28; ++i) { f32x2 d = xf[i] - mm; xf[i] = d; q2 += d * d; }
    float dl = mean - m;
    float var = red8(q2.x + q2.y) * (1.0f / 448.0f) - dl * dl;
    var = __builtin_fmaxf(var, 0.f);
    float sd = rbf(__builtin_amdgcn_sqrtf(rbf(rbf(var) + 1e-5f)));
    float r = __builtin_amdgcn_rcpf(sd);
#pragma unroll
    for (int j = 0; j < 7; ++j) {
      u32x4 o;
#pragma unroll
      for (int e = 0; e < 4; ++e) o[e] = cvtpk(rbf(xf[j * 4 + e].x) * r, rbf(xf[j * 4 + e].y) * r);
      *(u32x4*)(sm + row * A_STRIDE + (sub + 8 * j) * 16) = o;
    }
    if (p == 0) {
      WAITV(0);
      if (wave >= 4) {
#pragma unroll
        for (int i = 0; i < 2; ++i) {
          if (wq + 4 * i < 7) {
            u32x4 o = {rt[i][0] | (rt[i][1] << 16), rt[i][2] | (rt[i][3] << 16), rt[i][4] | (rt[i][5] << 16), rt[i][6] | (rt[i][7] << 16)};
            *(u32x4*)((char*)wdf + ((size_t)(b * 7 + wq + 4 * i) * 64 + lane) * 16) = o;
          }
        }
      }
      if (wave < 4) {
#pragma unroll
        for (int s = 0; s < 4; ++s) { glds(srcg + (size_t)s * 16 * F_, bdst + s * 2048); glds(srcu + (size_t)s * 16 * F_, bdst + s * 2048 + 1024); }
      }
      SB();
    }
  }
  SYNC();

  if (wave >= 4) {
    {
      const int gq = lane >> 4, q = (lane & 15) >> 2, p = lane & 3;
      const unsigned base = (32 * wq + 16 * (gq >> 1) + q) * A_STRIDE + (32 * nt + 16 * (gq & 1) + 4 * p) * 2;
      s16x4 v[4];
#pragma unroll
      for (int i = 0; i < 4; ++i) v[i] = __builtin_amdgcn_ds_read_tr16_b64_v4i16((LAS s16x4*)(sm + base + 4 * i * A_STRIDE));
      u16* dst = xnT + (size_t)(32 * nt + 16 * (gq & 1) + (lane & 15)) * L_ + mt * 128 + 32 * wq + 16 * (gq >> 1);
#pragma unroll
      for (int c = 0; c < 2; ++c) {
        u32x4 o;
#pragma unroll
        for (int e = 0; e < 4; ++e) {
          const s16x4 w = v[2 * c + (e >> 1)];
          o[e] = cvtpkh(bf2f((u16)w[2 * (e & 1)]), bf2f((u16)w[2 * (e & 1) + 1]));
        }
        *(u32x4*)(dst + 8 * c) = o;
      }
    }
    const float dt = rbf(exp_acc(bf2f(pl)));
    float wr[4], wi[4], mx = 0.f;
#pragma unroll
    for (int i = 0; i < 4; ++i) {
      float br = dt * bf2f(pb[i][0]), bi = dt * bf2f(pb[i][1]), cr = bf2f(pb[i][2]), ci = bf2f(pb[i][3]);
      wr[i] = cr * br - ci * bi; wi[i] = cr * bi + ci * br;
      mx = __builtin_fmaxf(mx, __builtin_fmaxf(__builtin_fabsf(wr[i]), __builtin_fabsf(wi[i])));
    }
    mx = wave_max(mx);
    unsigned eb = (F2U(mx) >> 23) & 255u;
    eb = eb < 2u ? 126u : (eb > 250u ? 250u : eb);
    const float sc = U2F((253u - eb) << 23);
    const float kscale = U2F((eb + 1u) << 23);
    KP k0 = kparams(dt, sc, pa[0][0], pa[0][1], wr[0], wi[0]);
    KP kp1 = kparams(dt, sc, pa[1][0], pa[1][1], wr[1], wi[1]);
    *(f32x4*)(kreg + hf * 2048 + lane * 32) = k0.p0;
    *(u32x2*)(kreg + hf * 2048 + lane * 32 + 16) = k0.p1;
    __builtin_amdgcn_wave_barrier();
    f32x4 kacc[2][2];
#pragma unroll
    for (int a = 0; a < 2; ++a)
#pragma unroll
      for (int bb = 0; bb < 2; ++bb) kacc[a][bb] = f32x4{0.f, 0.f, 0.f, 0.f};
    char* pbase = kreg + hf * 2048;
    kgen_steps<4>(pbase, lane, kacc);
    __builtin_amdgcn_wave_barrier();
    *(f32x4*)(pbase + lane * 32) = kp1.p0;
    *(u32x2*)(pbase + lane * 32 + 16) = kp1.p1;
    __builtin_amdgcn_wave_barrier();
    kgen_steps<2>(pbase, lane, kacc);
    char* tab = kreg + 4096;
    volatile LAS unsigned* flag = (volatile LAS unsigned*)(LAS char*)(sm + 896 + (wq >> 1) * 8);
    if (hf == 1) { tab_store(tab, lane, kacc); flag_set(flag); flag_wait(flag + 1); }
    else { flag_wait(flag); tab_add(tab, lane, kacc); tab_store(tab, lane, kacc); flag_set(flag + 1); }
    u16* kh = Kg + (size_t)dch * KL;
#pragma unroll
    for (int c = 0; c < 2; ++c) {
      u32x4 o;
#pragma unroll
      for (int e = 0; e < 4; ++e) {
        const unsigned to = tapo[c][e];
        o[e] = cvtpkh(*(const float*)(tab + (to & 0xffffu)), *(const float*)(tab + (to >> 16)));
      }
      *(u32x4*)(kh + hf * 1024 + lane * 16 + c * 8) = o;
    }
    if (hf == 0 && lane < 8) { u32x4 z = {0, 0, 0, 0}; *(u32x4*)(kh + 2048 + 8 * lane) = z; }
    if (hf == 1 && lane == 0) *(float*)(ws + WS_KS + dch * 4) = kscale;
    return;
  }

  const int h = lane >> 5, blk = (lane >> 4) & 1, q = (lane & 15) >> 2, p = lane & 3;
  const unsigned tra = G1_B_OFF + wave * 8192 + 64 * (8 * h + q) + 32 * blk + 8 * p;
  f32x16 acc[4][2];
  bf16x8 xf[2][4], wf_[2][2];
  unsigned xa = (lane & 31) * A_STRIDE + 16 * h;
  auto rdx = [&](int u, int buf, int tt) { xf[buf][tt] = *(const bf16x8*)(sm + xa + tt * 32 * A_STRIDE + u * 32); };
  auto rdw = [&](int u, int buf, int g) {
    s16x4 a0 = __builtin_amdgcn_ds_read_tr16_b64_v4i16((LAS s16x4*)(sm + tra + (u & 3) * 2048 + g * 1024));
    s16x4 a1 = __builtin_amdgcn_ds_read_tr16_b64_v4i16((LAS s16x4*)(sm + tra + (u & 3) * 2048 + g * 1024 + 256));
    wf_[buf][g] = __builtin_shufflevector(a0, a1, 0, 1, 2, 3, 4, 5, 6, 7);
  };
#define MM(tt, g) acc[tt][g] = MFMA32(wf_[u & 1][g], xf[u & 1][tt], acc[tt][g])
  WAITV(6);
  rdw(0, 0, 0); rdw(0, 0, 1); rdx(0, 0, 0); rdx(0, 0, 1); rdx(0, 0, 2); rdx(0, 0, 3);
  SB();
  const f32x16 zero16 = {0.f, 0.f, 0.f, 0.f, 0.f, 0.f, 0.f, 0.f, 0.f, 0.f, 0.f, 0.f, 0.f, 0.f, 0.f, 0.f};
#undef MM
#define MM(tt, g) acc[tt][g] = MFMA32(wf_[u & 1][g], xf[u & 1][tt], (FIRST && u == 0) ? zero16 : acc[tt][g])
#define G1_GROUP()                                                      \
  _Pragma("unroll") for (int u = 0; u < 4; ++u) {                       \
    const int nb = (u + 1) & 1;                                         \
    MM(0, 0); SB();                                                     \
    WAITV(4);                                                           \
    rdw(u + 1, nb, 0); rdw(u + 1, nb, 1);                               \
    SB();                                                               \
    MM(0, 1); SB();                                                     \
    glds(sg + (size_t)u * 16 * F_, bdst + u * 2048);                    \
    SB();                                                               \
    MM(1, 0); SB();                                                     \
    rdx(u + 1, nb, 0); rdx(u + 1, nb, 1);                               \
    SB();                                                               \
    MM(1, 1); SB();                                                     \
    glds(su + (size_t)u * 16 * F_, bdst + u * 2048 + 1024);             \
    SB();                                                               \
    MM(2, 0); SB();                                                     \
    rdx(u + 1, nb, 2); rdx(u + 1, nb, 3);                               \
    SB();                                                               \
    MM(2, 1); SB();                                                     \
    MM(3, 0); SB();                                                     \
    MM(3, 1); SB();                                                     \
  }                                                                     \
  xa += 128;
  {
    constexpr bool FIRST = true;
    const u16* sg = srcg + (size_t)64 * F_;
    const u16* su = srcu + (size_t)64 * F_;
    G1_GROUP();
  }
#pragma unroll 1
  for (int it = 1; it < 7; ++it) {
    constexpr bool FIRST = false;
    const int nx = it < 6 ? it + 1 : 6;
    const u16* sg = srcg + (size_t)nx * 64 * F_;
    const u16* su = srcu + (size_t)nx * 64 * F_;
    G1_GROUP();
  }
  WAITV(0);
#pragma unroll
  for (int tt = 0; tt < 4; ++tt)
#pragma unroll
    for (int s = 0; s < 2; ++s) {
      u32x4 o;
#pragma unroll
      for (int e = 0; e < 4; ++e)
        o[e] = cvtpk(rbf(acc[tt][0][8 * s + 2 * e]) * rbf(acc[tt][1][8 * s + 2 * e]), rbf(acc[tt][0][8 * s + 2 * e + 1]) * rbf(acc[tt][1][8 * s + 2 * e + 1]));
      const int mtile = 4 * mt + tt, kstep = 8 * nt + 2 * wave + s;
      st_wt((char*)hfrag + ((size_t)(mtile * 112 + kstep) * 64 + lane) * 16, o);
    }
}

constexpr int CS = 4160;
constexpr int CV_K_OFF = 512;
constexpr int CV_X_OFF = 33792;
constexpr int L2_LDS = 49152;
constexpr int POLL_SLEEP = 8;

__global__ __launch_bounds__(256, 3) void k_l2(const u16* __restrict__ x, const u16* __restrict__ Dskip, char* __restrict__ ws,
                                               u16* __restrict__ out) {
  extern __shared__ __attribute__((aligned(16))) char sm[];
  const int tid = TIDX, lane = tid & 63;
  const int wave = __builtin_amdgcn_readfirstlane(tid >> 6);
  u16* aT = (u16*)(ws + WS_AT);
  if (BIDX < 448) {
    const int d = BIDX;
    const u16* kh = (const u16*)(ws + WS_K) + (size_t)d * KL + tid * 8;
    const u16* xnT = (const u16*)(ws + WS_XT);
    {
      const u16* kh2 = (const u16*)(ws + WS_K2) + (size_t)d * KL + tid * 8;
      u32x4 ka = *(const u32x4*)(kh);
      u32x2 kb = *(const u32x2*)(kh + 8);
      const u32x4 ka2 = *(const u32x4*)(kh2);
      const u32x2 kb2 = *(const u32x2*)(kh2 + 8);
      u32x4 xv = *(const u32x4*)(xnT + (size_t)d * L_ + tid * 8);
      u32x4 z = {0, 0, 0, 0};
      if (tid < 160) *(u32x4*)(sm + CV_X_OFF + tid * 16) = z;
      if (tid < 16) *(u32x4*)(sm + CV_K_OFF + (tid >> 2) * CS + 4096 + (tid & 3) * 16) = z;
#pragma unroll
      for (int e = 0; e < 4; ++e) { const unsigned p = ka[e], q2 = ka2[e]; ka[e] = __builtin_bit_cast(unsigned, __builtin_bit_cast(f16x2v, p) + __builtin_bit_cast(f16x2v, q2)); }
#pragma unroll
      for (int e = 0; e < 2; ++e) { const unsigned p = kb[e], q2 = kb2[e]; kb[e] = __builtin_bit_cast(unsigned, __builtin_bit_cast(f16x2v, p) + __builtin_bit_cast(f16x2v, q2)); }
      const unsigned a10 = __builtin_amdgcn_alignbit(ka[1], ka[0], 16), a21 = __builtin_amdgcn_alignbit(ka[2], ka[1], 16);
      const unsigned a32 = __builtin_amdgcn_alignbit(ka[3], ka[2], 16), a43 = __builtin_amdgcn_alignbit(kb[0], ka[3], 16);
      const unsigned a54 = __builtin_amdgcn_alignbit(kb[1], kb[0], 16);
      char* dst = sm + CV_K_OFF + tid * 16;
      *(u32x4*)(dst) = ka;
      *(u32x4*)(dst + CS) = u32x4{a10, a21, a32, a43};
      *(u32x4*)(dst + 2 * CS) = u32x4{ka[1], ka[2], ka[3], kb[0]};
      *(u32x4*)(dst + 3 * CS) = u32x4{a21, a32, a43, a54};
      *(u32x4*)(sm + CV_X_OFF + (32 + (tid >> 2)) * 80 + (tid & 3) * 16) = xv;
    }
    const float ksc = *(const float*)(ws + WS_KS + d * 4);
    const float ds = bf2f(Dskip[d]);
    SYNC();
    const int r = lane & 31, h = lane >> 5;
    const int cls = (3 - r) & 3;
    unsigned ao = CV_K_OFF + cls * CS + 2 * (2047 - 32 * wave - r + 8 * h - cls) - 256;
    unsigned bo = CV_X_OFF + (32 + r - wave) * 80 + 16 * h - 320;
    f32x16 acc0, acc1;
#pragma unroll
    for (int i = 0; i < 16; ++i) { acc0[i] = 0.f; acc1[i] = 0.f; }
    bf16x8 Af[2], B1[2], B0[2];
    auto rd = [&](int nxt, int kk, int buf, bool both) {
      const unsigned a_ = ao + (nxt ? 0 : 256) + 32 * kk;
      s16x4 h0 = *(const volatile LAS s16x4*)(LAS char*)(sm + a_), h1 = *(const volatile LAS s16x4*)(LAS char*)(sm + a_ + 8);
      Af[buf] = __builtin_shufflevector(h0, h1, 0, 1, 2, 3, 4, 5, 6, 7);
      const unsigned b_ = bo + (nxt ? 0 : 320) + 32 * kk;
      B1[buf] = *(const bf16x8*)(sm + b_ + 2560);
      if (both) B0[buf] = *(const bf16x8*)(sm + b_);
    };
    rd(0, 0, 0, true);
    SB();
#pragma unroll 1
    for (int q = 0; q < 8; ++q) {
      rd(0, 1, 1, true); SB();
      acc1 = MFMA32H(Af[0], B1[0], acc1);
      acc0 = MFMA32H(Af[0], B0[0], acc0); SB();
      rd(1, 0, 0, true); SB();
      acc1 = MFMA32H(Af[1], B1[1], acc1);
      acc0 = MFMA32H(Af[1], B0[1], acc0); SB();
      ao -= 256; bo -= 320;
    }
#pragma unroll 1
    for (int q = 8; q < 16; ++q) {
      rd(0, 1, 1, false); SB();
      acc1 = MFMA32H(Af[0], B1[0], acc1); SB();
      rd(1, 0, 0, false); SB();
      acc1 = MFMA32H(Af[1], B1[1], acc1); SB();
      ao -= 256; bo -= 320;
    }
    SYNC();
#pragma unroll
    for (int g = 0; g < 4; ++g) {
      *(f32x4*)(sm + wave * 8192 + g * 1024 + lane * 16) = f32x4{acc0[4 * g], acc0[4 * g + 1], acc0[4 * g + 2], acc0[4 * g + 3]};
      *(f32x4*)(sm + wave * 8192 + 4096 + g * 1024 + lane * 16) = f32x4{acc1[4 * g], acc1[4 * g + 1], acc1[4 * g + 2], acc1[4 * g + 3]};
    }
    SYNC();
    {
      const int g = tid & 3, i = tid >> 2, tile = i >> 5, col = i & 31;
      f32x4 s0 = {0.f, 0.f, 0.f, 0.f}, s1 = {0.f, 0.f, 0.f, 0.f};
#pragma unroll
      for (int w = 0; w < 4; ++w) {
        s0 += *(const f32x4*)(sm + w * 8192 + tile * 4096 + g * 1024 + col * 16);
        s1 += *(const f32x4*)(sm + w * 8192 + tile * 4096 + g * 1024 + (col + 32) * 16);
      }
      u32x4 xv = *(const u32x4*)(sm + CV_X_OFF + (32 + i) * 80 + g * 16);
      float y[8] = {s0.x, s0.y, s0.z, s0.w, s1.x, s1.y, s1.z, s1.w};
      u32x4 o;
#pragma unroll
      for (int e = 0; e < 4; ++e) {
        const unsigned xe = xv[e];
        f16x2v xh = __builtin_bit_cast(f16x2v, xe);
        o[e] = cvtpk(rbf(y[2 * e] * ksc) + rbf((float)xh.x * ds), rbf(y[2 * e + 1] * ksc) + rbf((float)xh.y * ds));
      }
      st_wt(aT + (size_t)d * L_ + tid * 8, o);
    }
    WAITV(0);
    SYNC();
    if (tid == 0) __hip_atomic_fetch_add((unsigned*)(ws + WS_CNT + (d >> 6) * 256), 1u, __ATOMIC_RELAXED, __HIP_MEMORY_SCOPE_AGENT);
    return;
  }
  const u16* hfrag = (const u16*)(ws + WS_H);
  const u16* wdf = (const u16*)(ws + WS_WD);
  const int b = BIDX - 448;
  const int xcd = b & 7, idx = b >> 3;
  const int mt2 = xcd * 4 + (idx & 3), nt2 = idx >> 2;
  const u32x4* hp = (const u32x4*)((const char*)hfrag + ((size_t)(2 * mt2) * 112 + 28 * wave) * 1024) + lane;
  const u32x4* wp = (const u32x4*)((const char*)wdf + ((size_t)(2 * nt2) * 112 + 28 * wave) * 1024) + lane;
  f32x16 acc[2][2];
#pragma unroll
  for (int i = 0; i < 2; ++i)
#pragma unroll
    for (int j = 0; j < 2; ++j)
#pragma unroll
      for (int e = 0; e < 16; ++e) acc[i][j][e] = 0.f;
  const int t = 64 * mt2 + 32 * (wave & 1) + (lane & 31), h = lane >> 5;
  const int d0 = 64 * nt2 + 32 * (wave >> 1) + 4 * h;
  constexpr int DP = 4;
  u32x4 hb[DP][2], wb[DP][2];
#pragma unroll
  for (int ks = 0; ks < DP; ++ks) {
    hb[ks][0] = hp[ks * 64]; wb[ks][0] = wp[ks * 64]; wb[ks][1] = wp[(112 + ks) * 64]; hb[ks][1] = hp[(112 + ks) * 64];
  }
  u32x2 xv[4];
#pragma unroll
  for (int g = 0; g < 4; ++g) xv[g] = *(const u32x2*)(x + (size_t)t * D_ + d0 + 8 * g);
  SB();
#pragma unroll 1
  for (int it = 0; it < 6; ++it) {
    hp += DP * 64; wp += DP * 64;
#pragma unroll
    for (int sl = 0; sl < DP; ++sl) {
      acc[0][0] = MFMA32(BC8(wb[sl][0]), BC8(hb[sl][0]), acc[0][0]);
      acc[0][1] = MFMA32(BC8(wb[sl][1]), BC8(hb[sl][0]), acc[0][1]);
      acc[1][0] = MFMA32(BC8(wb[sl][0]), BC8(hb[sl][1]), acc[1][0]);
      acc[1][1] = MFMA32(BC8(wb[sl][1]), BC8(hb[sl][1]), acc[1][1]);
      SB();
      hb[sl][0] = hp[sl * 64]; wb[sl][0] = wp[sl * 64]; wb[sl][1] = wp[(112 + sl) * 64]; hb[sl][1] = hp[(112 + sl) * 64];
      SB();
    }
  }
#pragma unroll
  for (int sl = 0; sl < DP; ++sl) {
    acc[0][0] = MFMA32(BC8(wb[sl][0]), BC8(hb[sl][0]), acc[0][0]);
    acc[0][1] = MFMA32(BC8(wb[sl][1]), BC8(hb[sl][0]), acc[0][1]);
    acc[1][0] = MFMA32(BC8(wb[sl][0]), BC8(hb[sl][1]), acc[1][0]);
    acc[1][1] = MFMA32(BC8(wb[sl][1]), BC8(hb[sl][1]), acc[1][1]);
    SB();
  }
#pragma unroll
  for (int tl = 0; tl < 4; ++tl) {
    if (tl != wave) {
      const int slot = wave < tl ? wave : wave - 1;
#pragma unroll
      for (int g = 0; g < 4; ++g)
        *(f32x4*)(sm + (tl * 3 + slot) * 4096 + g * 1024 + lane * 16) =
            f32x4{acc[tl & 1][tl >> 1][4 * g], acc[tl & 1][tl >> 1][4 * g + 1], acc[tl & 1][tl >> 1][4 * g + 2], acc[tl & 1][tl >> 1][4 * g + 3]};
    }
  }
  if (wave == 0) {
    const unsigned* cnt = (const unsigned*)(ws + WS_CNT + nt2 * 256);
    for (unsigned spins = 0; spins < (1u << 20); ++spins) {
      unsigned c = __builtin_amdgcn_readfirstlane(__hip_atomic_load(cnt, __ATOMIC_RELAXED, __HIP_MEMORY_SCOPE_AGENT));
      if (c >= 64u) break;
      __builtin_amdgcn_s_sleep(POLL_SLEEP);
    }
  }
  SYNC();
  u16 av[16];
#pragma unroll
  for (int e = 0; e < 16; ++e)
    av[e] = __hip_atomic_load(aT + (size_t)(d0 + 8 * (e >> 2) + (e & 3)) * L_ + t, __ATOMIC_RELAXED, __HIP_MEMORY_SCOPE_AGENT);
  f32x16 own;
  if (wave == 0) own = acc[0][0]; else if (wave == 1) own = acc[1][0]; else if (wave == 2) own = acc[0][1]; else own = acc[1][1];
  float sum[16];
#pragma unroll
  for (int e = 0; e < 16; ++e) sum[e] = 0.f;
#pragma unroll
  for (int sw = 0; sw < 4; ++sw) {
    if (sw == wave) {
#pragma unroll
      for (int e = 0; e < 16; ++e) sum[e] += own[e];
    } else {
      const int slot = sw < wave ? sw : sw - 1;
#pragma unroll
      for (int g = 0; g < 4; ++g) {
        f32x4 v = *(const f32x4*)(sm + (wave * 3 + slot) * 4096 + g * 1024 + lane * 16);
        sum[4 * g] += v.x; sum[4 * g + 1] += v.y; sum[4 * g + 2] += v.z; sum[4 * g + 3] += v.w;
      }
    }
  }
#pragma unroll
  for (int g = 0; g < 4; ++g) {
    float o0 = rbf(lo16(xv[g].x) + bf2f(av[4 * g])) + rbf(sum[4 * g]);
    float o1 = rbf(hi16(xv[g].x) + bf2f(av[4 * g + 1])) + rbf(sum[4 * g + 1]);
    float o2 = rbf(lo16(xv[g].y) + bf2f(av[4 * g + 2])) + rbf(sum[4 * g + 2]);
    float o3 = rbf(hi16(xv[g].y) + bf2f(av[4 * g + 3])) + rbf(sum[4 * g + 3]);
    u32x2 o = {cvtpk(o0, o1), cvtpk(o2, o3)};
    *(u32x2*)(out + (size_t)t * D_ + d0 + 8 * g) = o;
  }
}

#if defined(__HIP_DEVICE_COMPILE__)
#pragma clang attribute pop
#endif

extern "C" void kernel_launch(void* const* d_in, const int* in_sizes, int n_in, void* d_out, int out_size, void* d_ws, size_t ws_size,
                              hipStream_t stream) {
  const u16* x = (const u16*)d_in[0];
  char* ws = (char*)d_ws;
  (void)hipFuncSetAttribute((const void*)k_l1, hipFuncAttributeMaxDynamicSharedMemorySize, G1_LDS);
  k_l1<<<256, 512, G1_LDS, stream>>>(x, (const u16*)d_in[10], (const u16*)d_in[11], (const u16*)d_in[12], (const u16*)d_in[1],
                                     (const u16*)d_in[2], (const u16*)d_in[3], (const u16*)d_in[4], (const u16*)d_in[5],
                                     (const u16*)d_in[6], (const u16*)d_in[7], ws);
  k_l2<<<672, 256, L2_LDS, stream>>>(x, (const u16*)d_in[8], ws, (u16*)d_out);
}
```
